```python
import torch

def _softmax(x, axis=-1):
    return torch.softmax(x, dim=axis)
def _gelu(x):
    return torch.nn.functional.gelu(x, approximate="tanh")
def _silu(x):
    return torch.nn.functional.silu(x)
def _sigmoid(x):
    return torch.sigmoid(x)
def _layernorm(x, axis=-1, eps=1e-5):
    m = x.mean(dim=axis, keepdim=True)
    v = x.var(dim=axis, keepdim=True, unbiased=False)
    return (x - m) / torch.sqrt(v + eps)
def _scatter_add(tgt, idx, src, axis=0):
    return tgt.clone().index_add_(axis, idx, src)
def _segment_sum(src, seg, n):
    out = torch.zeros((n,) + src.shape[1:], dtype=src.dtype)
    return out.index_add_(0, seg, src)
def _pad(x, pw):
    flat = tuple(v for pair in reversed(pw) for v in pair)
    return torch.nn.functional.pad(x, flat)

kernel_name = 'recurrent_scan__linear_attn_block__dense_activation_gelu__prompt'

def setup_inputs():

    _w = (lambda a: (lambda t: t.to(torch.bfloat16) if t.is_floating_point() else t)(torch.as_tensor(a)))
    def _A_setup():
        B, S, D = 1, 2048, 576
        return {
            "x": _w((lambda *s: torch.randn(*s, dtype=torch.bfloat16) * 0.05)(B, S, D)),
            "wqkv": _w((lambda *s: torch.randn(*s, dtype=torch.bfloat16) * 0.2)(D, 3 * D)),
            "wo": _w((lambda *s: torch.randn(*s, dtype=torch.bfloat16) * 0.2)(D, D)),
        }
    _A_in = _A_setup()

    def _B_setup():
        B, S, D, F = 1, 2048, 576, 4608
        return {
            "x": _w((lambda *s: torch.randn(*s, dtype=torch.bfloat16) * 0.05)(B, S, D)),
            "w1": _w((lambda *s: torch.randn(*s, dtype=torch.bfloat16) * 0.4)(D, F) * (D ** -0.5)),
            "b1": _w((lambda *s: torch.randn(*s, dtype=torch.bfloat16) * 12.8)(F) * 0.01),
            "w2": _w((lambda *s: torch.randn(*s, dtype=torch.bfloat16) * 0.2)(F, D) * (F ** -0.5)),
            "b2": _w((lambda *s: torch.randn(*s, dtype=torch.bfloat16) * 1.6)(D) * 0.01),
        }
    _B_in = _B_setup()

    return {
        "A_x": _A_in["x"],
        "A_wqkv": _A_in["wqkv"],
        "A_wo": _A_in["wo"],
        "B_x": _B_in["x"],
        "B_w1": _B_in["w1"],
        "B_b1": _B_in["b1"],
        "B_w2": _B_in["w2"],
        "B_b2": _B_in["b2"],
    }

def reference(A_x, A_wqkv, A_wo, B_x, B_w1, B_b1, B_w2, B_b2):
    def _A_ref(x, wqkv, wo):
        B, S, D = x.shape
        H = 32; Dh = D // H
        qkv = (lambda x, *d: x.permute(*d))((x @ wqkv).reshape(B, S, 3, H, Dh), 2, 0, 3, 1, 4)
        q, k, v = qkv[0], qkv[1], qkv[2]
        phi = lambda t: torch.where(t > 0, t + 1, torch.exp(t)) + 1e-6
        pq, pk = phi(q), phi(k)
        kv = torch.einsum("bhsd,bhse->bhde", pk, v)
        z = torch.einsum("bhsd->bhd", pk)
        num = torch.einsum("bhqd,bhde->bhqe", pq, kv)
        den = torch.einsum("bhqd,bhd->bhq", pq, z)[..., None] + 1e-6
        o = num / den
        return (lambda x, *d: x.permute(*d))(o, 0, 2, 1, 3).reshape(B, S, D) @ wo

    def _B_ref(x, w1, b1, w2, b2):
        h = torch.einsum("bsd,df->bsf", x, w1) + b1
        h = _gelu(h)
        return torch.einsum("bsf,fd->bsd", h, w2) + b2

    x = A_x
    xn = _layernorm(x, axis=-1)
    s = _A_ref(xn, A_wqkv, A_wo)
    g = _silu(_B_ref(xn, B_w1, B_b1, B_w2, B_b2))
    return x + s * g
```

```cpp
#include <hip/hip_runtime.h>
#include <hip/hip_bf16.h>
typedef unsigned short u16;
__device__ __forceinline__ float bf2f(u16 v){ return __uint_as_float(((unsigned)v)<<16); }
__device__ __forceinline__ u16 f2bf(float f){ unsigned u=__float_as_uint(f); u += 0x7fffu + ((u>>16)&1u); return (u16)(u>>16); }
__device__ __forceinline__ float rbf(float f){ return bf2f(f2bf(f)); }
constexpr int S=2048, D=576, F=4608, H=32, DH=18;

__global__ void n_ln(const u16* x, float* xn){
  int r=blockIdx.x; __shared__ float red[64*2];
  float s=0; for(int i=threadIdx.x;i<D;i+=64) s+=bf2f(x[r*D+i]);
  for(int o=32;o;o>>=1) s+=__shfl_xor(s,o);
  float m=s/D; float q=0; for(int i=threadIdx.x;i<D;i+=64){float d=bf2f(x[r*D+i])-m; q+=d*d;}
  for(int o=32;o;o>>=1) q+=__shfl_xor(q,o);
  float rs=1.0f/sqrtf(q/D+1e-5f);
  for(int i=threadIdx.x;i<D;i+=64) xn[r*D+i]=rbf((bf2f(x[r*D+i])-m)*rs);
}
__global__ void n_gemm(const float* A, const u16* B, float* C, int M,int N,int K){
  int n=blockIdx.x*64+threadIdx.x; int m0=blockIdx.y*8;
  if(n>=N) return;
  float acc[8]={0,0,0,0,0,0,0,0};
  for(int k=0;k<K;k++){ float b=bf2f(B[(size_t)k*N+n]);
    #pragma unroll
    for(int i=0;i<8;i++) acc[i]+=A[(size_t)(m0+i)*K+k]*b; }
  for(int i=0;i<8;i++) C[(size_t)(m0+i)*N+n]=acc[i];
}
__device__ __forceinline__ float phi(float t){ return (t>0? t+1.0f: __expf(t))+1e-6f; }
__global__ void n_kv(const float* qkv, float* kv, float* z){
  int h=blockIdx.x; int t=threadIdx.x; if(t>=DH*(DH+1)) return; int d=t/(DH+1), e=t%(DH+1);
  float a=0; for(int s=0;s<S;s++){ float pk=rbf(phi(rbf(qkv[(size_t)s*3*D+D+h*DH+d]))); float v=(e<DH)? rbf(qkv[(size_t)s*3*D+2*D+h*DH+e]):1.0f; a+=pk*v; }
  if(e<DH) kv[(h*DH+d)*DH+e]=a; else z[h*DH+d]=a;
}
__global__ void n_o(const float* qkv,const float* kv,const float* z,float* o){
  int s=blockIdx.x; int c=threadIdx.x; if(c>=D) return; int h=c/DH,e=c%DH;
  float num=0,den=0; for(int d=0;d<DH;d++){ float pq=rbf(phi(rbf(qkv[(size_t)s*3*D+h*DH+d]))); num+=pq*kv[(h*DH+d)*DH+e]; den+=pq*z[h*DH+d]; }
  o[(size_t)s*D+c]=rbf(num/(den+1e-6f));
}
__global__ void n_gelu(float* h,const u16* b1){
  size_t i=(size_t)blockIdx.x*256+threadIdx.x; int f=i%F; float v=h[i]+bf2f(b1[f]);
  float u=0.7978845608f*(v+0.044715f*v*v*v); h[i]=rbf(0.5f*v*(1.0f+tanhf(u)));
}
__global__ void n_fin(const u16* x,const float* s,const float* y,const u16* b2,u16* out){
  size_t i=(size_t)blockIdx.x*256+threadIdx.x; int c=i%D; float v=y[i]+bf2f(b2[c]); float g=v/(1.0f+__expf(-v));
  out[i]=f2bf(bf2f(x[i])+s[i]*g);
}
extern "C" void kernel_launch(void* const* d_in, const int* in_sizes, int n_in, void* d_out, int out_size, void* d_ws, size_t ws_size, hipStream_t stream){
  const u16* x=(const u16*)d_in[0]; const u16* wqkv=(const u16*)d_in[1]; const u16* wo=(const u16*)d_in[2];
  const u16* w1=(const u16*)d_in[4]; const u16* b1=(const u16*)d_in[5]; const u16* w2=(const u16*)d_in[6]; const u16* b2=(const u16*)d_in[7];
  float* ws=(float*)d_ws; float* xn=ws; float* qkv=xn+S*D; float* kv=qkv+(size_t)S*3*D; float* z=kv+H*DH*DH; float* o=z+H*DH+64; float* sA=o+S*D; float* h=sA+S*D; float* y=h+(size_t)S*F;
  n_ln<<<S,64,0,stream>>>(x,xn);
  n_gemm<<<dim3(3*D/64,S/8),64,0,stream>>>(xn,wqkv,qkv,S,3*D,D);
  n_kv<<<H,384,0,stream>>>(qkv,kv,z);
  n_o<<<S,576,0,stream>>>(qkv,kv,z,o);
  n_gemm<<<dim3(D/64,S/8),64,0,stream>>>(o,wo,sA,S,D,D);
  n_gemm<<<dim3(F/64,S/8),64,0,stream>>>(xn,w1,h,S,F,D);
  n_gelu<<<S*F/256,256,0,stream>>>(h,b1);
  n_gemm<<<dim3(D/64,S/8),64,0,stream>>>(h,w2,y,S,D,F);
  n_fin<<<S*D/256,256,0,stream>>>(x,sA,y,b2,(u16*)d_out);
}
```

```cpp
#include <hip/hip_runtime.h>
#include <hip/hip_bf16.h>

typedef unsigned short u16;
typedef unsigned long long u64;
typedef short s16x4 __attribute__((ext_vector_type(4)));
typedef __bf16 bf16x8 __attribute__((ext_vector_type(8)));
typedef __bf16 bf16x2 __attribute__((ext_vector_type(2)));
typedef float f32x2 __attribute__((ext_vector_type(2)));
typedef float f32x4 __attribute__((ext_vector_type(4)));
typedef float f32x16 __attribute__((ext_vector_type(16)));
typedef unsigned u32x2 __attribute__((ext_vector_type(2)));
typedef unsigned u32x4 __attribute__((ext_vector_type(4)));
typedef __amdgpu_buffer_rsrc_t rsrc_t;
#define LAS __attribute__((address_space(3)))
#if defined(__HIP_DEVICE_COMPILE__)
#define NOPK __attribute__((target("no-packed-fp32-ops")))
#else
#define NOPK
#endif
#define DI __device__ __forceinline__ NOPK
#define BAR() __builtin_amdgcn_s_barrier()
#define SB() __builtin_amdgcn_sched_barrier(0)
#define CB() asm volatile("" ::: "memory")
#define SGB(m, n) __builtin_amdgcn_sched_group_barrier(m, n, 0)
#define WAITV(n) asm volatile("s_waitcnt vmcnt(%0)" ::"i"(n) : "memory")
#define WL(n) __builtin_amdgcn_s_waitcnt(0xC07F | ((n) << 8))
#define MFMA32(a, b, c) __builtin_amdgcn_mfma_f32_32x32x16_bf16(a, b, c, 0, 0, 0)
#define MFMA16(a, b, c) __builtin_amdgcn_mfma_f32_16x16x32_bf16(a, b, c, 0, 0, 0)
#define SC1 16

constexpr unsigned WS_EPOCH = 0;
constexpr unsigned WS_FKV1 = 4096;
constexpr unsigned WS_FKV2 = WS_FKV1 + 2048;
constexpr unsigned WS_FO = WS_FKV2 + 2048;
constexpr unsigned WS_FY = WS_FO + 2048;
constexpr unsigned WS_FREP = WS_FY + 2048;
constexpr unsigned WS_WQP = 16384;
constexpr unsigned WQP_J = 4 * 18 * 4096;
constexpr unsigned WS_KVP = WS_WQP + 8 * WQP_J;
constexpr unsigned KVP_WG = 73 * 288;
constexpr unsigned WS_KVF = WS_KVP + 256 * KVP_WG;
constexpr unsigned WS_OF = WS_KVF + 8 * 12288;
constexpr unsigned WS_YP = WS_OF + 32 * 73728;
constexpr unsigned WS_END = WS_YP + 32 * 8 * 8 * 10240;
constexpr int RING_OFF = 73728, SLOT_B = 24576, BIAS_OFF = 147456, PQ_OFF = 149760, DUMMY_OFF = 162048, LDS_BYTES = 163840;

DI float bf2f(u16 v) { return __builtin_bit_cast(float, ((unsigned)v) << 16); }
DI float bflo(unsigned v) { return __builtin_bit_cast(float, v << 16); }
DI float bfhi(unsigned v) { return __builtin_bit_cast(float, v & 0xffff0000u); }
DI unsigned pk2(float a, float b) { f32x2 v = {a, b}; bf16x2 r = __builtin_convertvector(v, bf16x2); return __builtin_bit_cast(unsigned, r); }
DI int wrap9(int v) { return v >= 9 ? v - 9 : v; }
DI rsrc_t mkrs(const void* p, unsigned bytes) { return __builtin_amdgcn_make_buffer_rsrc((void*)p, 0, bytes, 0x00020000); }
DI void dma16(rsrc_t rs, unsigned lds, int vo, int so) {
  asm volatile("s_mov_b32 m0, %0\n\ts_nop 0\n\tbuffer_load_dwordx4 %1, %2, %3 offen lds" ::"s"(lds), "v"(vo), "s"(rs), "s"(so) : "memory");
}
DI s16x4 trd(LAS char* p, int off) { return __builtin_amdgcn_ds_read_tr16_b64_v4i16((LAS s16x4*)(p + off)); }
DI bf16x8 cat(s16x4 a, s16x4 b) { return __builtin_bit_cast(bf16x8, __builtin_shufflevector(a, b, 0, 1, 2, 3, 4, 5, 6, 7)); }
DI void set4(f32x16& v, int i, f32x4 x) { v[4 * i] = x[0]; v[4 * i + 1] = x[1]; v[4 * i + 2] = x[2]; v[4 * i + 3] = x[3]; }
DI u32x4 pack8(const f32x16& p, int t) {
  return u32x4{pk2(p[8 * t], p[8 * t + 1]), pk2(p[8 * t + 2], p[8 * t + 3]), pk2(p[8 * t + 4], p[8 * t + 5]), pk2(p[8 * t + 6], p[8 * t + 7])};
}
DI void aload16(u32x4& d, rsrc_t rs, int vo, int so) { asm volatile("buffer_load_dwordx4 %0, %1, %2, %3 offen sc1" : "=v"(d) : "v"(vo), "s"(rs), "s"(so) : "memory"); }
DI void aload8(u32x2& d, rsrc_t rs, int vo, int so) { asm volatile("buffer_load_dwordx2 %0, %1, %2, %3 offen sc1" : "=v"(d) : "v"(vo), "s"(rs), "s"(so) : "memory"); }
#define PIN(x) asm volatile("" : "+v"(x))
DI bool tag_ok(u32x2 v, u32x2 tag, bool act) { return __builtin_amdgcn_ballot_w64(act && (v[0] != tag[0] || v[1] != tag[1])) == 0; }
DI void poll_block(rsrc_t rs, int vo, int so, u32x2 tag, bool act) {
  for (int it = 0; it < 20000; ++it) {
    u32x2 v; aload8(v, rs, vo, so); WAITV(0); PIN(v);
    if (tag_ok(v, tag, act)) break;
    __builtin_amdgcn_s_sleep(2);
  }
}
DI void st_flag(rsrc_t rs, int so, u32x2 tag, bool on) { __builtin_amdgcn_raw_buffer_store_b64(tag, rs, on ? 0 : 0x7ffffff0, so, SC1); }

struct Params { const u16 *x, *wqkv, *wo, *w1, *b1, *w2, *b2; u16* out; char* ws; };

struct CS {
  bf16x8 B[36]; s16x4 A[4][4]; f32x16 acc;
  LAS char* an[2];
  LAS char* ap[2];
  LAS char* lb;
  LAS char* hr;
  LAS char* biasp;
  rsrc_t rws; int vy; int vof;
  bool pm, one;
};
struct GS { float x[16]; float a[16]; unsigned pk[8]; };
template <int EPI, int E, int ST> DI void epi_op(const f32x16& prev, GS& g, bool pm, bool one) {
  if constexpr (EPI == 1) {
    if constexpr (ST == 0) { float x = prev[E]; g.x[E] = x; g.a[E] = __builtin_fmaf(x * x, -0.1029432397f, -2.302208198f); }
    if constexpr (ST == 1) g.a[E] = __builtin_amdgcn_exp2f(g.x[E] * g.a[E]);
    if constexpr (ST == 2) g.a[E] = __builtin_amdgcn_rcpf(1.0f + g.a[E]);
    if constexpr (ST == 3) g.x[E] = g.x[E] * g.a[E];
  } else {
    if constexpr (ST == 0) { float x = prev[E]; g.x[E] = x; g.a[E] = __builtin_amdgcn_exp2f(x * 1.44269504089f); }
    if constexpr (ST == 1) { float x = g.x[E]; g.a[E] = (x > 0.f ? x + 1.0f : g.a[E]); }
    if constexpr (ST == 2) { float p = g.a[E] + 1e-6f; g.x[E] = pm ? p : g.x[E]; }
    if constexpr (ST == 3) g.x[E] = one ? 1.0f : g.x[E];
  }
  if constexpr (ST == 4 && (E & 1)) g.pk[E >> 1] = pk2(g.x[E - 1], g.x[E]);
}
template <int EPI, int T, int NUM, int HF, int E = 0> DI void epi_step(const f32x16& prev, GS& g, bool pm, bool one) {
  if constexpr (E < 16) {
    constexpr int st = T - (E * NUM) / 16;
    if constexpr (st >= 0 && st < 5 && ((st >= 3) == (HF == 0))) epi_op<EPI, E, st>(prev, g, pm, one);
    epi_step<EPI, T, NUM, HF, E + 1>(prev, g, pm, one);
  }
}
template <int B> DI void frag_write(GS& g, LAS char* p) { *(LAS u32x4*)p = u32x4{g.pk[B], g.pk[B + 1], g.pk[B + 2], g.pk[B + 3]}; }
constexpr int rst(int u) { constexpr int R[7] = {0, 5, 10, 15, 20, 24, 24}; return R[u]; }
template <int I, int IE, int SL, int PAR, int IMG> DI void do_reads(CS& s) {
  if constexpr (I < IE) {
    constexpr int uu = I / 4, q = I % 4, ab = (SL * 6 + uu + 2 * PAR) % 4;
    constexpr int o = (SL == 2 ? 0 : SL * SLOT_B) + uu * 4096;
    if constexpr (IMG == 0) s.A[ab][q] = trd(s.an[SL == 2], o + (q >> 1) * 2048 + (q & 1) * 512);
    else s.A[ab][q] = trd(s.ap[SL == 2], o + q * 1024);
    do_reads<I + 1, IE, SL, PAR, IMG>(s);
  }
}
struct PC { int boff; int w0, w1c; int y0, y1; bool cpm, cone; };
struct PCfg { int img, ori, pori, init, epi, cepi, hasprev, loadb, loado; };
template <int G, int PAR, int IMG, int ORI, int PORI, int INIT, int EPI, int CEPI, int HASPREV, int LOADB, int LOADO, int WPIN>
DI void do_gap(const PC& pc, CS& s, GS& g, f32x16& prev, f32x16& binit) {
  if constexpr (G < 36) {
    constexpr int ks = G / 2, hf = G % 2, sl = ks / 6, u = ks % 6;
    constexpr int km = (ks + 16) % 18, abm = (ks + 2 + 2 * PAR) % 4;
    constexpr bool wr_prev = (sl == 1 && CEPI != 0) || (sl == 0 && WPIN);
    if constexpr (u == 0 && hf == 0) { if constexpr (wr_prev) WL(1); else WL(0); CB(); BAR(); CB(); SB(); }
    if constexpr (hf == 0 && u >= 2) { constexpr int n = rst(u) - 4 * (u - 2) - 5 + (LOADB ? 4 : 0); WL(n > 15 ? 15 : n); SB(); }
    if constexpr (ks >= 2 || HASPREV) {
      f32x16 c;
      if constexpr (ks == 2 && hf == 0) { if constexpr (INIT) c = binit; else for (int i = 0; i < 16; i++) c[i] = 0.f; } else c = s.acc;
      const bf16x8 a = cat(s.A[abm][2 * hf], s.A[abm][2 * hf + 1]);
      f32x16 d;
      if constexpr ((ks < 2 ? PORI : ORI) == 0) d = MFMA32(a, s.B[2 * km + hf], c); else d = MFMA32(s.B[2 * km + hf], a, c);
      if constexpr (ks == 1 && hf == 1) prev = d; else s.acc = d;
    }
    if constexpr (hf == 0) do_reads<rst(u), rst(u + 1), sl, PAR, IMG>(s);
    if constexpr (INIT && G == 1) {
#pragma unroll
      for (int m = 0; m < 4; m++) set4(binit, m, *(LAS f32x4*)(s.biasp + pc.boff + m * 32));
    }
    if constexpr (LOADB && hf == 1) { s.B[2 * ks] = *(LAS bf16x8*)(s.hr + (2 * ks) * 1024); s.B[2 * ks + 1] = *(LAS bf16x8*)(s.hr + (2 * ks + 1) * 1024); }
    if constexpr (CEPI != 0 && ks < 4) epi_step<CEPI, ks + 16, 16, hf>(prev, g, pc.cpm, pc.cone);
    if constexpr ((EPI == 1 || EPI == 2) && ks >= 2) epi_step<EPI, ks - 2, LOADB ? 5 : 16, hf>(prev, g, s.pm, s.one);
    if constexpr (CEPI != 0 && G == 10) frag_write<4>(g, s.lb + pc.w1c);
    if constexpr ((EPI == 1 || EPI == 2) && !LOADB && G == 34) frag_write<0>(g, s.lb + pc.w0);
    if constexpr (LOADB && G == 22) { frag_write<0>(g, s.lb + pc.w0); frag_write<4>(g, s.lb + pc.w0 + 1024); }
    if constexpr (EPI == 3 && G == 7) __builtin_amdgcn_raw_buffer_store_b128(pack8(prev, 0), s.rws, s.vy, pc.y0, SC1);
    if constexpr (EPI == 3 && G == 9) __builtin_amdgcn_raw_buffer_store_b128(pack8(prev, 1), s.rws, s.vy, pc.y1, SC1);
    SGB(0x8, 1); SGB(0x100, 8); SGB(0x200, 2); SGB(0x2, 24); SGB(0x40, 1);
    SB();
    if constexpr (LOADO && hf == 1 && ks >= 2 && ks < 11) {
      u32x4 t0, t1; aload16(t0, s.rws, s.vof, (2 * (ks - 2)) * 1024); aload16(t1, s.rws, s.vof, (2 * (ks - 2) + 1) * 1024);
      s.B[2 * (ks - 2)] = __builtin_bit_cast(bf16x8, t0); s.B[2 * (ks - 2) + 1] = __builtin_bit_cast(bf16x8, t1); SB();
    }
    do_gap<G + 1, PAR, IMG, ORI, PORI, INIT, EPI, CEPI, HASPREV, LOADB, LOADO, WPIN>(pc, s, g, prev, binit);
  }
}
#define PASS(PAR, IMG, ORI, PORI, INIT, EPI, CEPI, HASPREV, LOADB, LOADO, WPIN) do_gap<0, PAR, IMG, ORI, PORI, INIT, EPI, CEPI, HASPREV, LOADB, LOADO, WPIN>(pc, s, g, prev, binit)

DI int yoff(int b, int r, int j) {
  const int q = b / 9, b9 = b - 9 * q; const int hi = b9 >= 5; const int jo = 2 * q + hi, be = b9 - 5 * hi;
  return WS_YP + (((r * 8 + jo) * 8 + j) * 5 + be) * 2048;
}

struct SS { LAS char* a[5]; s16x4 A[2][5][2]; f32x4 acc[5]; };
template <int SG> DI void s_steps(CS& s, SS& q, rsrc_t rws, int fy_so, u32x2 tag, int lane, int wid) {
  if constexpr (SG <= 18) {
    if constexpr (SG < 18 && SG % 3 == 0) {
      if constexpr (SG == 3) WAITV(0);
      WL(0); CB(); BAR(); CB(); SB();
      if constexpr (SG == 3) st_flag(rws, fy_so, tag, wid == 0 && lane == 0);
    }
    if constexpr (SG == 1) { WAITV(2); for (int i = 0; i < 18; i++) PIN(s.B[i]); SB(); }
    if constexpr (SG >= 1) {
#pragma unroll
      for (int n = 0; n < 5; n++) {
        f32x4 c; if constexpr (SG == 1) c = f32x4{0.f, 0.f, 0.f, 0.f}; else c = q.acc[n];
        q.acc[n] = MFMA16(cat(q.A[(SG - 1) & 1][n][0], q.A[(SG - 1) & 1][n][1]), s.B[SG - 1], c);
      }
    }
    if constexpr (SG < 18) {
#pragma unroll
      for (int n = 0; n < 5; n++) { q.A[SG & 1][n][0] = trd(q.a[n], (SG % 9) * 8192 - (SG % 9 >= 6 ? 49152 : 0) + 0); q.A[SG & 1][n][1] = trd(q.a[n], (SG % 9) * 8192 - (SG % 9 >= 6 ? 49152 : 0) + 1024); }
      if constexpr (SG % 9 == 5) { for (int n = 0; n < 5; n++) q.a[n] += 49152; }
      if constexpr (SG % 9 == 8) { for (int n = 0; n < 5; n++) q.a[n] -= 49152; }
    }
    SB();
    s_steps<SG + 1>(s, q, rws, fy_so, tag, lane, wid);
  }
}

DI void consumer(const Params& p, LAS char* smem, int wid, int lane, int r, int j, int rot, u32x2 tag) {
  CS s; GS g; f32x16 prev, binit; PC pc;
  const int th = wid & 1, cw = wid >> 1;
  const int gq = lane >> 4, q = (lane >> 2) & 3, pp = lane & 3, h = lane >> 5, g2 = gq & 1;
  {
    const int c = 4 * cw + 2 * g2 + (pp >> 1);
    s.an[0] = smem + RING_OFF + h * 1024 + q * 128 + ((c ^ (((q >> 1) & 1) << 2)) * 16) + (pp & 1) * 8;
    s.ap[0] = smem + RING_OFF + (4 * h + q) * 128 + ((c ^ (((q >> 1) & 1) << 2)) * 16) + (pp & 1) * 8;
    s.an[1] = s.an[0] + 2 * SLOT_B; s.ap[1] = s.ap[0] + 2 * SLOT_B; PIN(s.an[1]); PIN(s.ap[1]);
    s.lb = smem + lane * 16; s.hr = s.lb + th * 36864; s.biasp = smem + BIAS_OFF + cw * 128 + h * 16;
    s.rws = mkrs(p.ws, WS_END);
    s.vy = (2 * th + g2) * 512 + h * 256 + (lane & 15) * 16;
    s.vof = WS_OF + (r * 4 + wid) * 18432 + lane * 16;
    s.pm = true; s.one = false;
  }
#pragma unroll
  for (int k = 0; k < 36; k++) s.B[k] = *(LAS bf16x8*)(smem + th * 36864 + k * 1024 + ((lane ^ (2 * (k & 3) + h)) * 16));
  const int hb = th * 36864 + cw * 2048;
  const int kb = th * 36864 + 24576;
  const int qb = PQ_OFF + th * 6144;
  pc.cpm = true; pc.cone = false; pc.y0 = pc.y1 = 0;
  pc.boff = wrap9(rot) * 256; pc.w0 = 0; pc.w1c = 0;                       PASS(0, 0, 0, 0, 1, 0, 0, 0, 0, 0, 0);
  pc.boff = wrap9(1 + rot) * 256; pc.w0 = hb;                              PASS(1, 0, 0, 0, 1, 1, 0, 1, 0, 0, 0);
  pc.boff = wrap9(2 + rot) * 256; pc.w0 = hb + 4096; pc.w1c = hb + 1024;   PASS(0, 0, 0, 0, 1, 1, 1, 1, 0, 0, 1);
  pc.w0 = hb + 8192; pc.w1c = hb + 4096 + 1024;                            PASS(1, 0, 1, 0, 0, 1, 1, 1, 0, 0, 1);
  s.pm = (cw == 0); pc.w0 = kb + cw * 2048; pc.w1c = hb + 8192 + 1024;     PASS(0, 0, 1, 1, 0, 2, 1, 1, 0, 0, 1);
  pc.cpm = s.pm; pc.w1c = pc.w0 + 1024; pc.w0 = kb + 4096 + cw * 2048;     PASS(1, 0, 1, 1, 0, 2, 2, 1, 0, 0, 1);
  pc.w1c = pc.w0 + 1024; pc.w0 = cw == 0 ? qb + 4096 : DUMMY_OFF;
  s.pm = ((lane + 8) & 31) < 16; s.one = (lane & 31) == 16;                PASS(0, 0, 0, 1, 0, 2, 2, 1, 0, 0, 1);
  pc.cpm = s.pm; pc.cone = s.one; pc.w1c = cw == 0 ? pc.w0 + 1024 : DUMMY_OFF; pc.w0 = qb + cw * 2048; s.pm = true; s.one = false;
  pc.boff = wrap9(3 + rot) * 256;                                          PASS(1, 0, 0, 0, 1, 2, 2, 1, 0, 0, 1);
  pc.cpm = true; pc.cone = false; pc.w1c = pc.w0 + 1024; pc.w0 = hb + 3 * 4096;
  pc.boff = wrap9(4 + rot) * 256;                                          PASS(0, 0, 0, 0, 1, 1, 2, 1, 0, 0, 1);
  for (int i = 5; i < 9; i += 2) {
    pc.w1c = pc.w0 + 1024; pc.w0 = hb + (i - 1) * 4096; pc.boff = wrap9(i + rot) * 256;      PASS(1, 0, 0, 0, 1, 1, 1, 1, 0, 0, 1);
    pc.w1c = pc.w0 + 1024; pc.w0 = hb + i * 4096; pc.boff = wrap9(i + 1 + rot) * 256;        PASS(0, 0, 0, 0, 1, 1, 1, 1, 0, 0, 1);
  }
  pc.w1c = pc.w0 + 1024; pc.w0 = hb + 8 * 4096;                            PASS(1, 1, 0, 0, 0, 1, 1, 1, 1, 0, 1);
  for (int i = 1; i < 7; i += 2) {
    { const int b = 4 * wrap9(i - 1 + rot) + 2 * cw; pc.y0 = yoff(b, r, j); pc.y1 = yoff(b + 1, r, j); }   PASS(0, 1, 0, 0, 0, 3, 0, 1, 0, 0, 0);
    { const int b = 4 * wrap9(i + rot) + 2 * cw; pc.y0 = yoff(b, r, j); pc.y1 = yoff(b + 1, r, j); }       PASS(1, 1, 0, 0, 0, 3, 0, 1, 0, 0, 0);
  }
  u32x2 fo; aload8(fo, s.rws, (lane & 7) * 8, WS_FO + r * 64);
  { const int b = 4 * wrap9(6 + rot) + 2 * cw; pc.y0 = yoff(b, r, j); pc.y1 = yoff(b + 1, r, j); }         PASS(0, 1, 0, 0, 0, 3, 0, 1, 0, 0, 0);
  WAITV(2); PIN(fo);
  if (!tag_ok(fo, tag, true)) poll_block(s.rws, (lane & 7) * 8, WS_FO + r * 64, tag, true);
  if (r == 0 && j == 0 && wid == 0) st_flag(s.rws, WS_EPOCH, tag, lane == 0);
  { const int b = 4 * wrap9(7 + rot) + 2 * cw; pc.y0 = yoff(b, r, j); pc.y1 = yoff(b + 1, r, j); }         PASS(1, 1, 0, 0, 0, 3, 0, 1, 0, 1, 0);
  {
    f32x16 d = MFMA32(cat(s.A[2][0], s.A[2][1]), s.B[32], s.acc); d = MFMA32(cat(s.A[2][2], s.A[2][3]), s.B[33], d);
    d = MFMA32(cat(s.A[3][0], s.A[3][1]), s.B[34], d); d = MFMA32(cat(s.A[3][2], s.A[3][3]), s.B[35], d);
    const int b = 4 * wrap9(8 + rot) + 2 * cw;
    __builtin_amdgcn_raw_buffer_store_b128(pack8(d, 0), s.rws, s.vy, yoff(b, r, j), SC1);
    __builtin_amdgcn_raw_buffer_store_b128(pack8(d, 1), s.rws, s.vy, yoff(b + 1, r, j), SC1);
    SB();
  }
  SS sq;
  const int c0 = 288 * (j >> 1) + (j & 1) * 160;
  {
    const int rho = 4 * g2 + q; const int wo0 = c0 & 127;
#pragma unroll
    for (int n = 0; n < 5; n++) { const int o = wo0 + 32 * n; const int lc = o >> 7, tau = (o >> 5) & 3;
      sq.a[n] = smem + RING_OFF + lc * 4096 + (gq >> 1) * 2048 + rho * 128 + (((2 * tau + (pp >> 1)) ^ (((rho >> 1) & 3) << 1)) * 16) + (pp & 1) * 8; }
  }
  s_steps<0>(s, sq, s.rws, WS_FY + (r * 8 + j) * 8, tag, lane, wid);
  const int tok = r * 64 + wid * 16 + (lane & 15);
  const rsrc_t rx = mkrs(p.x, 2048 * 1152), rb2 = mkrs(p.b2, 1152), rout = mkrs(p.out, 2048 * 1152);
  u32x2 xv[5], bv[5];
#pragma unroll
  for (int n = 0; n < 5; n++) { xv[n] = __builtin_amdgcn_raw_buffer_load_b64(rx, tok * 1152 + gq * 8, c0 + 32 * n, 0); bv[n] = __builtin_amdgcn_raw_buffer_load_b64(rb2, gq * 8, c0 + 32 * n, 0); }
  poll_block(s.rws, (lane & 7) * 8, WS_FY + r * 64, tag, true);
  u32x4 yv[3][8];
  const int ybase = WS_YP + (r * 8 + j) * 8 * 10240;
  const int vyl = (gq >> 1) * 2048 + wid * 512 + g2 * 256 + (lane & 15) * 16, vyl2 = wid * 512 + g2 * 256 + (lane & 15) * 16;
#pragma unroll
  for (int i = 0; i < 8; i++) {
    yv[0][i] = __builtin_amdgcn_raw_buffer_load_b128(s.rws, vyl, ybase + i * 10240, SC1);
    yv[1][i] = __builtin_amdgcn_raw_buffer_load_b128(s.rws, vyl, ybase + i * 10240 + 4096, SC1);
    yv[2][i] = __builtin_amdgcn_raw_buffer_load_b128(s.rws, vyl2, ybase + i * 10240 + 8192, SC1);
  }
#pragma unroll
  for (int pi = 0; pi < 3; pi++) {
    float lo[4], hi[4];
#pragma unroll
    for (int e = 0; e < 4; e++) { lo[e] = 0.f; hi[e] = 0.f; }
#pragma unroll
    for (int i = 0; i < 8; i++) {
      lo[0] += bflo(yv[pi][i][0]); lo[1] += bfhi(yv[pi][i][0]); lo[2] += bflo(yv[pi][i][1]); lo[3] += bfhi(yv[pi][i][1]);
      hi[0] += bflo(yv[pi][i][2]); hi[1] += bfhi(yv[pi][i][2]); hi[2] += bflo(yv[pi][i][3]); hi[3] += bfhi(yv[pi][i][3]);
    }
#pragma unroll
    for (int e = 0; e < 4; e++) {
      auto sw = __builtin_amdgcn_permlane32_swap(__builtin_bit_cast(unsigned, lo[e]), __builtin_bit_cast(unsigned, hi[e]), false, false);
      lo[e] = __builtin_bit_cast(float, (unsigned)sw[0]); hi[e] = __builtin_bit_cast(float, (unsigned)sw[1]);
    }
#pragma unroll
    for (int t = 0; t < 2; t++) {
      const int n = 2 * pi + t;
      if (n < 5) {
        float o[4];
#pragma unroll
        for (int e = 0; e < 4; e++) {
          const unsigned bw = bv[n][e >> 1], xw = xv[n][e >> 1];
          const float y = (t ? hi[e] : lo[e]) + ((e & 1) ? bfhi(bw) : bflo(bw));
          const float gt = y * __builtin_amdgcn_rcpf(1.0f + __builtin_amdgcn_exp2f(-1.44269504089f * y));
          o[e] = __builtin_fmaf(sq.acc[n][e], gt, (e & 1) ? bfhi(xw) : bflo(xw));
        }
        if (n < 4 || (j & 1) == 0) __builtin_amdgcn_raw_buffer_store_b64(u32x2{pk2(o[0], o[1]), pk2(o[2], o[3])}, rout, tok * 1152 + gq * 8, c0 + 32 * n, 0);
      }
    }
  }
}

struct LS { rsrc_t r1, r2, rw, ro; int vo1, vo2, voq, voo, voo2; unsigned d; int j, rot, line0; };
DI void issue_slot(const LS& c, int i) {
  const int t = i % 3; const unsigned d = c.d + t * SLOT_B;
  if (i < 9 || (i >= 21 && i < 39)) {
    const int pi = (i < 9 ? i : i - 12) / 3; const int so = c.j * 1152 + wrap9(pi + c.rot) * 128 + t * 192 * 9216;
#pragma unroll
    for (int u = 0; u < 6; u++) dma16(c.r1, d + u * 4096, c.vo1, so + u * 32 * 9216);
  } else if (i < 21) {
    const int so = WS_WQP + c.j * WQP_J + (i - 9) * SLOT_B;
#pragma unroll
    for (int u = 0; u < 6; u++) dma16(c.rw, d + u * 4096, c.voq, so + u * 4096);
  } else if (i < 66) {
    const int L = wrap9((i - 39) / 3 + c.rot);
#pragma unroll
    for (int u = 0; u < 6; u += 2) { const int so = (c.j * 576 + wrap9(t * 3 + (u >> 1) + c.rot) * 64) * 1152 + L * 128;
      dma16(c.r2, d + u * 4096, c.vo2, so); dma16(c.r2, d + (u + 1) * 4096, c.vo2, so + 32 * 1152); }
  } else {
#pragma unroll
    for (int u = 0; u < 3; u++) { const int sg = (i - 66) * 3 + u;
      const int so = (sg < 16 ? (72 * (sg >> 1) + 32 * (sg & 1)) : 288 * (sg - 16)) * 1152 + c.line0 * 128; const int vo = sg < 16 ? c.voo : c.voo2;
      dma16(c.ro, d + u * 8192, vo, so); dma16(c.ro, d + u * 8192 + 4096, vo, so + 128); }
  }
}
#define LSTEP(i, n) do { WAITV(n); BAR(); CB(); issue_slot(c, (i) + 2); } while (0)

DI void loader(const Params& p, LAS char* smem, LS& c, int lw, int lane, int r, u32x2 tag) {
  const int j = c.j; const rsrc_t rws = c.rw;
  const int h = lane >> 5, e32 = lane & 31;
  BAR(); CB(); issue_slot(c, 2);
  for (int i = 1; i < 22; i++) LSTEP(i, 6);
  LSTEP(22, 6); LSTEP(23, 6);
  {
    const int ta0 = lw == 0 ? 0 : (lw == 3 ? 4 : 2), tb0 = lw == 0 ? 1 : (lw == 1 ? 1 : (lw == 2 ? 4 : 3));
    const int ta1 = lw == 1 ? 2 : (lw == 3 ? 4 : 0), tb1 = lw < 2 ? 3 : 4;
    LAS char* lb = smem + lane * 16;
#pragma unroll
    for (int pr = 0; pr < 2; pr++) {
      const int ta = pr ? ta1 : ta0, tb = pr ? tb1 : tb0;
      const int oa = ta == 4 ? PQ_OFF + 4096 : 24576 + ta * 2048, sa = ta == 4 ? 6144 : 36864;
      const int ob = tb == 4 ? PQ_OFF + 4096 : 24576 + tb * 2048, sb = tb == 4 ? 6144 : 36864;
      f32x16 d; for (int i = 0; i < 16; i++) d[i] = 0.f;
#pragma unroll
      for (int k = 0; k < 4; k++)
        d = MFMA32(*(LAS bf16x8*)(lb + oa + (k >> 1) * sa + (k & 1) * 1024), *(LAS bf16x8*)(lb + ob + (k >> 1) * sb + (k & 1) * 1024), d);
      const int e = tb == 4 ? (e32 == 16 ? 72 : (e32 >= 8 && e32 < 16 ? 56 + e32 : -1)) : (tb >> 1) * 32 + e32;
      const int hd = e / 18;
#pragma unroll
      for (int m = 0; m < 4; m++) { const int d0 = (ta >> 1) * 32 + 8 * m + 4 * h;
        const bool need = e >= 0 && d0 < 72 && (e == 72 || (d0 + 3 >= 18 * hd && d0 < 18 * hd + 18));
        const int vo = need ? e * 288 + d0 * 4 : 0x7ffffff0;
        __builtin_amdgcn_raw_buffer_store_b128(__builtin_bit_cast(u32x4, f32x4{d[4 * m], d[4 * m + 1], d[4 * m + 2], d[4 * m + 3]}), rws, vo, WS_KVP + (j * 32 + r) * KVP_WG, SC1); }
    }
  }
  LSTEP(24, 14); LSTEP(25, 14); LSTEP(26, 6);
  st_flag(rws, WS_FKV1 + (j * 32 + r) * 8, tag, lw == 0 && lane == 0);
  LSTEP(27, 7); LSTEP(28, 7); LSTEP(29, 6);
  u32x2 fl; aload8(fl, rws, e32 * 8, WS_FKV1 + j * 256);
  LSTEP(30, 7); LSTEP(31, 7); WAITV(12); PIN(fl);
  if (!tag_ok(fl, tag, true)) poll_block(rws, e32 * 8, WS_FKV1 + j * 256, tag, true);
  {
    const int pi = lane & 15, sg = lane >> 4;
    const int P = r * 48 + lw * 12 + (pi < 12 ? pi : 11);
    const int f = P >> 7, l2 = (P & 127) >> 1, half = P & 1, rho = l2 & 31, hh = l2 >> 5;
    const int T = f < 3 ? 0 : (f < 7 ? 1 : 2), ks = f < 3 ? f : (f < 7 ? f - 2 : f - 7);
    const int d0 = 16 * ks + 8 * half + 4 * hh;
    const int row = T < 2 ? 32 * T + rho : (rho < 8 ? 64 + rho : 72);
    const int hd = T < 2 ? row / 18 : (rho < 8 ? 3 : (rho < 16 ? (rho & 3) : -9));
    const bool need = d0 + 3 >= 18 * hd && d0 < 18 * hd + 18;
    const int vo = need ? sg * 8 * KVP_WG + row * 288 + d0 * 4 : 0x7ffffff0;
    u32x4 v[8];
#pragma unroll
    for (int i = 0; i < 8; i++) aload16(v[i], rws, vo, WS_KVP + (j * 32 + i) * KVP_WG);
    LSTEP(32, 14); LSTEP(33, 14); LSTEP(34, 6);
    float a[4];
#pragma unroll
    for (int i = 0; i < 8; i++) { PIN(v[i]);
#pragma unroll
      for (int e = 0; e < 4; e++) { const unsigned w = v[i][e]; const float fv = __builtin_bit_cast(float, w); a[e] = i == 0 ? fv : a[e] + fv; } }
#pragma unroll
    for (int e = 0; e < 4; e++) {
      auto s1 = __builtin_amdgcn_permlane16_swap(__builtin_bit_cast(unsigned, a[e]), __builtin_bit_cast(unsigned, a[e]), false, false);
      a[e] = __builtin_bit_cast(float, (unsigned)s1[0]) + __builtin_bit_cast(float, (unsigned)s1[1]);
      auto s2 = __builtin_amdgcn_permlane32_swap(__builtin_bit_cast(unsigned, a[e]), __builtin_bit_cast(unsigned, a[e]), false, false);
      a[e] = __builtin_bit_cast(float, (unsigned)s2[0]) + __builtin_bit_cast(float, (unsigned)s2[1]);
      const int d = d0 + e; a[e] = (d >= 18 * hd && d < 18 * hd + 18) ? a[e] : 0.f;
    }
    __builtin_amdgcn_raw_buffer_store_b64(u32x2{pk2(a[0], a[1]), pk2(a[2], a[3])}, rws, lane < 12 ? P * 8 : 0x7ffffff0, WS_KVF + j * 12288, SC1);
  }
  LSTEP(35, 7); LSTEP(36, 7); LSTEP(37, 6);
  st_flag(rws, WS_FKV2 + (j * 32 + r) * 8, tag, lw == 0 && lane == 0);
  LSTEP(38, 7); LSTEP(39, 7); LSTEP(40, 6);
  aload8(fl, rws, e32 * 8, WS_FKV2 + j * 256);
  LSTEP(41, 7); LSTEP(42, 7); WAITV(12); PIN(fl);
  if (!tag_ok(fl, tag, true)) poll_block(rws, e32 * 8, WS_FKV2 + j * 256, tag, true);
  {
    const int th = lw & 1, cc = lw >> 1;
    u32x4 a[9];
#pragma unroll
    for (int i = 0; i < 4; i++) aload16(a[i], rws, lane * 16, WS_KVF + j * 12288 + (cc * 3 + i) * 1024);
#pragma unroll
    for (int i = 0; i < 5; i++) aload16(a[4 + i], rws, lane * 16, WS_KVF + j * 12288 + (7 + i) * 1024);
    LSTEP(43, 15); LSTEP(44, 15); LSTEP(45, 6);
    bf16x8 b[5];
#pragma unroll
    for (int i = 0; i < 4; i++) b[i] = *(LAS bf16x8*)(smem + PQ_OFF + th * 6144 + i * 1024 + lane * 16);
    b[4] = cat(trd(smem + PQ_OFF + th * 6144 + 4096 + ((lane >> 4) & 1) * 1024 + (24 + 4 * h + ((lane >> 2) & 3) + 32 * (lane & 1)) * 16 + ((lane >> 1) & 1) * 8, 0), s16x4{0, 0, 0, 0});
#pragma unroll
    for (int i = 0; i < 9; i++) PIN(a[i]);
    f32x16 d2, de; for (int i = 0; i < 16; i++) { d2[i] = 0.f; de[i] = 0.f; }
#pragma unroll
    for (int i = 0; i < 5; i++) d2 = MFMA32(__builtin_bit_cast(bf16x8, a[4 + i]), b[i], d2);
    if (cc == 0) {
#pragma unroll
      for (int i = 0; i < 3; i++) de = MFMA32(__builtin_bit_cast(bf16x8, a[i]), b[i], de);
    } else {
#pragma unroll
      for (int i = 0; i < 4; i++) de = MFMA32(__builtin_bit_cast(bf16x8, a[i]), b[i + 1], de);
    }
    float rd[4];
#pragma unroll
    for (int i = 0; i < 4; i++) rd[i] = 1.0f / (d2[4 + i] + 1e-6f);
#pragma unroll
    for (int e = 0; e < 16; e++) { const int ee = cc * 32 + (e & 3) + 8 * (e >> 2) + 4 * h;
      const float rr = ee < 18 ? rd[0] : (ee < 36 ? rd[1] : (ee < 54 ? rd[2] : rd[3])); de[e] *= rr; }
    const int ob = WS_OF + (r * 4 + 2 * th) * 18432;
    const int vo = ((lane >> 4) & 1) * 18432 + ((lane & 15) + 16 * h) * 16;
    __builtin_amdgcn_raw_buffer_store_b128(pack8(de, 0), rws, vo, ob + (2 * j + cc) * 1024, SC1);
    __builtin_amdgcn_raw_buffer_store_b128(pack8(de, 1), rws, vo, ob + (2 * j + cc) * 1024 + 512, SC1);
    const int vo2 = cc == 0 ? ((lane >> 4) & 1) * 18432 + ((lane & 15) + 16 * (j & 3)) * 16 + 8 * h : 0x7ffffff0;
    __builtin_amdgcn_raw_buffer_store_b64(u32x2{pk2(d2[0] * rd[3], d2[1] * rd[3]), pk2(d2[2] * rd[3], d2[3] * rd[3])}, rws, vo2, ob + (16 + (j >> 2)) * 1024, SC1);
  }
  LSTEP(46, 9); LSTEP(47, 9); LSTEP(48, 6);
  st_flag(rws, WS_FO + (r * 8 + j) * 8, tag, lw == 0 && lane == 0);
  LSTEP(49, 7); LSTEP(50, 7);
  for (int i = 51; i < 70; i++) LSTEP(i, 6);
  WAITV(6); BAR();
  WAITV(0); BAR();
}

__global__ __launch_bounds__(512, 2) NOPK void fused_main(Params p) {
  extern __shared__ __attribute__((aligned(16))) char smem_[];
  LAS char* smem = (LAS char*)smem_; const unsigned sm = (unsigned)(size_t)smem;
  const int tid = threadIdx.x, lane = tid & 63; const int wid = __builtin_amdgcn_readfirstlane(tid >> 6);
  const int j = blockIdx.x & 7, r = blockIdx.x >> 3; const int rot = r % 9;
  const int tau = lane >> 3, li = lane & 7;
  const rsrc_t rx = mkrs(p.x, 2048 * 1152);
  u32x4 xv[9];
#pragma unroll
  for (int m = 0; m < 9; m++) xv[m] = __builtin_amdgcn_raw_buffer_load_b128(rx, (r * 64 + wid * 8 + tau) * 1152 + li * 16, m * 128, 0);
  LS c;
  c.rw = mkrs(p.ws, WS_END);
  const u32x2 ep = __builtin_amdgcn_raw_buffer_load_b64(c.rw, 0, WS_EPOCH, SC1);
  const float bv0 = bf2f(p.b1[j * 576 + tid]); const float bv1 = bf2f(p.b1[j * 576 + 512 + (tid & 63)]);
  {
    const int lw = wid & 3; const int c1 = li ^ (((tau >> 1) & 1) << 2), c2 = li ^ (((tau >> 1) & 3) << 1);
    c.r1 = mkrs(p.w1, 576 * 9216); c.r2 = mkrs(p.w2, 4608 * 1152); c.ro = mkrs(p.wo, 576 * 1152);
    c.vo1 = (8 * lw + tau) * 9216 + c1 * 16; c.vo2 = (8 * lw + tau) * 1152 + c1 * 16; c.voq = lw * 1024 + lane * 16;
    c.voo = (8 * lw + tau) * 1152 + c2 * 16;
    c.voo2 = (72 * (2 * (lw >> 1) + (tau >> 2)) + 64 + 4 * (lw & 1) + (tau & 3)) * 1152 + c2 * 16;
    c.d = sm + RING_OFF + lw * 1024; c.j = j; c.rot = rot; c.line0 = (288 * (j >> 1) + (j & 1) * 160) >> 7;
    if (wid >= 4) { issue_slot(c, 0); issue_slot(c, 1); }
  }
  {
    float v[72]; float sum = 0.f;
#pragma unroll
    for (int m = 0; m < 9; m++)
#pragma unroll
      for (int e = 0; e < 4; e++) { v[8 * m + 2 * e] = bflo(xv[m][e]); v[8 * m + 2 * e + 1] = bfhi(xv[m][e]); sum += v[8 * m + 2 * e] + v[8 * m + 2 * e + 1]; }
#define RED8(x) do { x += __builtin_bit_cast(float, __builtin_amdgcn_update_dpp(0, __builtin_bit_cast(int, x), 0xB1, 0xf, 0xf, true)); \
                     x += __builtin_bit_cast(float, __builtin_amdgcn_update_dpp(0, __builtin_bit_cast(int, x), 0x4E, 0xf, 0xf, true)); \
                     x += __builtin_bit_cast(float, __builtin_amdgcn_update_dpp(0, __builtin_bit_cast(int, x), 0x141, 0xf, 0xf, true)); } while (0)
    RED8(sum);
    const float mean = sum * (1.0f / 576.0f); float sq = 0.f;
#pragma unroll
    for (int i = 0; i < 72; i++) { v[i] -= mean; sq = __builtin_fmaf(v[i], v[i], sq); }
    RED8(sq);
    const float rstd = 1.0f / __builtin_sqrtf(sq * (1.0f / 576.0f) + 1e-5f);
    WAITV(0);
    const int tk = wid * 8 + tau;
    LAS char* wb = smem + (tk >> 5) * 36864 + (li >> 1) * 1024 + ((((tk & 31) + 32 * (li & 1)) ^ li) * 16);
#pragma unroll
    for (int m = 0; m < 9; m++)
      *(LAS u32x4*)(wb + m * 4096) = u32x4{pk2(v[8 * m] * rstd, v[8 * m + 1] * rstd), pk2(v[8 * m + 2] * rstd, v[8 * m + 3] * rstd), pk2(v[8 * m + 4] * rstd, v[8 * m + 5] * rstd), pk2(v[8 * m + 6] * rstd, v[8 * m + 7] * rstd)};
    *(LAS float*)(smem + BIAS_OFF + tid * 4) = bv0; if (tid < 64) *(LAS float*)(smem + BIAS_OFF + 2048 + tid * 4) = bv1;
  }
  u32x2 tag; { const u64 e = ((u64)ep[1] << 32 | ep[0]) + 1; tag[0] = __builtin_amdgcn_readfirstlane((unsigned)e); tag[1] = __builtin_amdgcn_readfirstlane((unsigned)(e >> 32)); }
  WL(0); CB(); BAR(); CB();
  if (wid < 4) consumer(p, smem, wid, lane, r, j, rot, tag); else loader(p, smem, c, wid - 4, lane, r, tag);
}

__global__ __launch_bounds__(256) void repack_k(const u16* __restrict__ wqkv, char* __restrict__ ws) {
  const int idx = blockIdx.x * 256 + threadIdx.x;
  const int cp = idx & 7, rho = (idx >> 3) & 7, rg = (idx >> 6) & 3; const int u = idx >> 8; const int ks = u % 18, ps = (u / 18) & 3, j = u / 72;
  const int c = cp ^ (((rho >> 1) & 1) << 2); const int cw = c >> 2, c4 = c & 3;
  int col;
  if (ps == 0) col = (cw ? 1152 : 576) + 72 * j + 8 * c4;
  else if (ps == 1) col = (cw ? 1152 : 576) + 72 * j + 32 + 8 * c4;
  else if (ps == 2) col = (cw || c4 == 2) ? -1 : (c4 == 0 ? 576 : (c4 == 1 ? 1152 : 0)) + 72 * j + 64;
  else col = 72 * j + 32 * cw + 8 * c4;
  u32x4 v = {0u, 0u, 0u, 0u};
  if (col >= 0) v = *(const u32x4*)(wqkv + (size_t)(32 * ks + 8 * rg + rho) * 1728 + col);
  *(u32x4*)(ws + WS_WQP + (size_t)idx * 16) = v;
}

extern "C" void kernel_launch(void* const* d_in, const int* in_sizes, int n_in, void* d_out, int out_size, void* d_ws, size_t ws_size, hipStream_t stream) {
  Params p;
  p.x = (const u16*)d_in[0]; p.wqkv = (const u16*)d_in[1]; p.wo = (const u16*)d_in[2]; p.w1 = (const u16*)d_in[4]; p.b1 = (const u16*)d_in[5];
  p.w2 = (const u16*)d_in[6]; p.b2 = (const u16*)d_in[7]; p.out = (u16*)d_out; p.ws = (char*)d_ws;
  (void)hipFuncSetAttribute((const void*)fused_main, hipFuncAttributeMaxDynamicSharedMemorySize, LDS_BYTES);
  repack_k<<<576, 256, 0, stream>>>(p.wqkv, p.ws);
  fused_main<<<256, 512, LDS_BYTES, stream>>>(p);
}
```

```cpp
#include <hip/hip_runtime.h>
#include <hip/hip_bf16.h>

typedef unsigned short u16;
typedef unsigned long long u64;
typedef short s16x4 __attribute__((ext_vector_type(4)));
typedef __bf16 bf16x8 __attribute__((ext_vector_type(8)));
typedef __bf16 bf16x2 __attribute__((ext_vector_type(2)));
typedef float f32x2 __attribute__((ext_vector_type(2)));
typedef float f32x4 __attribute__((ext_vector_type(4)));
typedef float f32x16 __attribute__((ext_vector_type(16)));
typedef unsigned u32x2 __attribute__((ext_vector_type(2)));
typedef unsigned u32x4 __attribute__((ext_vector_type(4)));
typedef __amdgpu_buffer_rsrc_t rsrc_t;
#define LAS __attribute__((address_space(3)))
#if defined(__HIP_DEVICE_COMPILE__)
#define NOPK __attribute__((target("no-packed-fp32-ops")))
#else
#define NOPK
#endif
#define DI __device__ __forceinline__ NOPK
#define BAR() __builtin_amdgcn_s_barrier()
#define SB() __builtin_amdgcn_sched_barrier(0)
#define CB() asm volatile("" ::: "memory")
#define SGB(m, n) __builtin_amdgcn_sched_group_barrier(m, n, 0)
#define WAITV(n) asm volatile("s_waitcnt vmcnt(%0)" ::"i"(n) : "memory")
#define WL(n) __builtin_amdgcn_s_waitcnt(0xC07F | ((n) << 8))
#define MFMA32(a, b, c) __builtin_amdgcn_mfma_f32_32x32x16_bf16(a, b, c, 0, 0, 0)
#define MFMA16(a, b, c) __builtin_amdgcn_mfma_f32_16x16x32_bf16(a, b, c, 0, 0, 0)
#define SC1 16

constexpr unsigned WS_EPOCH = 0;
constexpr unsigned WS_FKV1 = 4096;
constexpr unsigned WS_FKV2 = WS_FKV1 + 4096;
constexpr unsigned WS_FREP = WS_FKV2 + 4096;
constexpr unsigned WS_FO = WS_FREP + 4096;
constexpr unsigned WS_FY = WS_FO + 4096;
constexpr unsigned WS_WQP = 32768;
constexpr unsigned WQP_J = 4 * 18 * 4096;
constexpr unsigned WS_KVP = WS_WQP + 8 * WQP_J;
constexpr unsigned KVP_WG = 32768;
constexpr unsigned WS_KVF = WS_KVP + 256 * KVP_WG;
constexpr unsigned WS_OF = WS_KVF + 8 * 12288;
constexpr unsigned WS_YP = WS_OF + 32 * 73728;
constexpr unsigned WS_END = WS_YP + 32 * 8 * 8 * 10240;
#define MAPMODE 1
#define SVPRIO 2
#define NG1A 4
#define RLPRIO 0
constexpr int RING_OFF = 73728, SLOT_B = 24576, BIAS_OFF = 147456, PQ_OFF = 149760, DUMMY_OFF = 162048, LDS_BYTES = 163840;

DI float bf2f(u16 v) { return __builtin_bit_cast(float, ((unsigned)v) << 16); }
DI float bflo(unsigned v) { return __builtin_bit_cast(float, v << 16); }
DI float bfhi(unsigned v) { return __builtin_bit_cast(float, v & 0xffff0000u); }
DI unsigned pk2(float a, float b) { f32x2 v = {a, b}; bf16x2 r = __builtin_convertvector(v, bf16x2); return __builtin_bit_cast(unsigned, r); }
DI int wrap9(int v) { return v >= 9 ? v - 9 : v; }
DI rsrc_t mkrs(const void* p, unsigned bytes) { return __builtin_amdgcn_make_buffer_rsrc((void*)p, 0, bytes, 0x00020000); }
#define DMA_NEXT(POL) "s_add_u32 m0, m0, 0x400\n\ts_add_u32 %0, %0, %4\n\tbuffer_load_dwordx4 %2, %3, %0 offen " POL "lds\n\t"
#define DMA_FIRST(POL) "s_mov_b32 m0, %1\n\ts_nop 0\n\tbuffer_load_dwordx4 %2, %3, %0 offen " POL "lds\n\t"
template <int N, int AG> DI void dma_run(rsrc_t rs, unsigned lds, int vo, int so, int step) {
  if constexpr (N == 8 && !AG) asm volatile(DMA_FIRST("") DMA_NEXT("") DMA_NEXT("") DMA_NEXT("") DMA_NEXT("") DMA_NEXT("") DMA_NEXT("") DMA_NEXT("") : "+s"(so) : "s"(lds), "v"(vo), "s"(rs), "s"(step) : "memory", "scc");
  if constexpr (N == 8 && AG) asm volatile(DMA_FIRST("sc1 ") DMA_NEXT("sc1 ") DMA_NEXT("sc1 ") DMA_NEXT("sc1 ") DMA_NEXT("sc1 ") DMA_NEXT("sc1 ") DMA_NEXT("sc1 ") DMA_NEXT("sc1 ") : "+s"(so) : "s"(lds), "v"(vo), "s"(rs), "s"(step) : "memory", "scc");
  if constexpr (N == 4) asm volatile(DMA_FIRST("") DMA_NEXT("") DMA_NEXT("") DMA_NEXT("") : "+s"(so) : "s"(lds), "v"(vo), "s"(rs), "s"(step) : "memory", "scc");
  if constexpr (N == 3) asm volatile(DMA_FIRST("sc1 ") DMA_NEXT("sc1 ") DMA_NEXT("sc1 ") : "+s"(so) : "s"(lds), "v"(vo), "s"(rs), "s"(step) : "memory", "scc");
  if constexpr (N == 2) asm volatile(DMA_FIRST("") DMA_NEXT("") : "+s"(so) : "s"(lds), "v"(vo), "s"(rs), "s"(step) : "memory", "scc");
}
DI s16x4 trd(LAS char* p, int off) { return __builtin_amdgcn_ds_read_tr16_b64_v4i16((LAS s16x4*)(p + off)); }
DI bf16x8 cat(s16x4 a, s16x4 b) { return __builtin_bit_cast(bf16x8, __builtin_shufflevector(a, b, 0, 1, 2, 3, 4, 5, 6, 7)); }
DI void set4(f32x16& v, int i, f32x4 x) { v[4 * i] = x[0]; v[4 * i + 1] = x[1]; v[4 * i + 2] = x[2]; v[4 * i + 3] = x[3]; }
DI u32x4 pack8(const f32x16& p, int t) {
  return u32x4{pk2(p[8 * t], p[8 * t + 1]), pk2(p[8 * t + 2], p[8 * t + 3]), pk2(p[8 * t + 4], p[8 * t + 5]), pk2(p[8 * t + 6], p[8 * t + 7])};
}
DI void aload16(u32x4& d, rsrc_t rs, int vo, int so) { asm volatile("buffer_load_dwordx4 %0, %1, %2, %3 offen sc1" : "=v"(d) : "v"(vo), "s"(rs), "s"(so) : "memory"); }
DI void aload8(u32x2& d, rsrc_t rs, int vo, int so) { asm volatile("buffer_load_dwordx2 %0, %1, %2, %3 offen sc1" : "=v"(d) : "v"(vo), "s"(rs), "s"(so) : "memory"); }
DI void aload8p(u32x2& d, rsrc_t rs, int vo, int so) { asm volatile("buffer_load_dwordx2 %0, %1, %2, %3 offen" : "=v"(d) : "v"(vo), "s"(rs), "s"(so) : "memory"); }
constexpr int OOB = 0x7ffffff0;
#define PIN(x) asm volatile("" : "+v"(x))
DI bool tag_ok(u32x2 v, u64 tag, unsigned need) {
  const u64 d = (((u64)v[1] << 32) | v[0]) - tag;
  return __builtin_amdgcn_ballot_w64(d < need || d > 15) == 0;
}
DI void poll_block(rsrc_t rs, int vo, int so, u64 tag, unsigned need) {
  for (int it = 0; it < 20000; ++it) {
    u32x2 v; aload8(v, rs, vo, so); WAITV(0); PIN(v);
    if (tag_ok(v, tag, need)) break;
    __builtin_amdgcn_s_sleep(2);
  }
}
DI void st_flag(rsrc_t rs, int so, u64 v, bool on) { __builtin_amdgcn_raw_buffer_store_b64(u32x2{(unsigned)v, (unsigned)(v >> 32)}, rs, on ? 0 : OOB, so, SC1); }

#define STAMP(i)
#define STAMPC(i)
#define STAMP_OUT(w)
struct Params { const u16 *x, *wqkv, *wo, *w1, *b1, *w2, *b2; u16* out; char* ws; };

struct CS {
  bf16x8 B[36]; s16x4 A[4][4]; f32x16 acc;
  LAS char* an[2];
  LAS char* ap[2];
  LAS char* lb;
  LAS char* hr;
  LAS char* biasp;
  rsrc_t rws; int vy;
  bool pm, one;
};
struct GS { float x[16]; float a[16]; unsigned pk[8]; };
template <int EPI, int E, int ST> DI void epi_op(const f32x16& prev, GS& g, bool pm, bool one) {
  if constexpr (EPI == 1) {
    if constexpr (ST == 0) { float x = prev[E]; g.x[E] = x; g.a[E] = __builtin_fmaf(x * x, -0.1029432397f, -2.302208198f); }
    if constexpr (ST == 1) g.a[E] = __builtin_amdgcn_exp2f(g.x[E] * g.a[E]);
    if constexpr (ST == 2) g.a[E] = __builtin_amdgcn_rcpf(1.0f + g.a[E]);
    if constexpr (ST == 3) g.x[E] = g.x[E] * g.a[E];
  } else {
    if constexpr (ST == 0) { float x = prev[E]; g.x[E] = x; g.a[E] = __builtin_amdgcn_exp2f(x * 1.44269504089f); }
    if constexpr (ST == 1) { float x = g.x[E]; g.a[E] = (x > 0.f ? x + 1.0f : g.a[E]); }
    if constexpr (ST == 2) { float p = g.a[E] + 1e-6f; g.x[E] = pm ? p : g.x[E]; }
    if constexpr (ST == 3) g.x[E] = one ? 1.0f : g.x[E];
  }
  if constexpr (ST == 4 && (E & 1)) g.pk[E >> 1] = pk2(g.x[E - 1], g.x[E]);
}
template <int EPI, int T, int NUM, int HF, int E = 0> DI void epi_step(const f32x16& prev, GS& g, bool pm, bool one) {
  if constexpr (E < 16) {
    constexpr int st = T - (E * NUM) / 16;
    if constexpr (st >= 0 && st < 5 && ((st >= 3) == (HF == 0))) epi_op<EPI, E, st>(prev, g, pm, one);
    epi_step<EPI, T, NUM, HF, E + 1>(prev, g, pm, one);
  }
}
template <int B> DI void frag_write(GS& g, LAS char* p) { *(LAS u32x4*)p = u32x4{g.pk[B], g.pk[B + 1], g.pk[B + 2], g.pk[B + 3]}; }
constexpr int rst(int u) { constexpr int R[7] = {0, 5, 10, 15, 20, 24, 24}; return R[u]; }
template <int I, int IE, int SL, int PAR, int IMG> DI void do_reads(CS& s) {
  if constexpr (I < IE) {
    constexpr int uu = I / 4, q = I % 4, ab = (SL * 6 + uu + 2 * PAR) % 4;
    constexpr int o = (SL == 2 ? 0 : SL * SLOT_B) + uu * 4096;
    if constexpr (IMG == 0) s.A[ab][q] = trd(s.an[SL == 2], o + (q >> 1) * 2048 + (q & 1) * 512);
    else s.A[ab][q] = trd(s.ap[SL == 2], o + q * 1024);
    do_reads<I + 1, IE, SL, PAR, IMG>(s);
  }
}
struct PC { int boff; int w0, w1c; int y0, y1; bool cpm, cone; u64 yf; int yfo; bool l0; };
struct PCfg { int img, ori, pori, init, epi, cepi, hasprev, loadb, loado; };
template <int G, int PAR, int IMG, int ORI, int PORI, int INIT, int EPI, int CEPI, int HASPREV, int LOADB, int YW, int WPIN>
DI void do_gap(const PC& pc, CS& s, GS& g, f32x16& prev, f32x16& binit) {
  if constexpr (G < 36) {
    constexpr int ks = G / 2, hf = G % 2, sl = ks / 6, u = ks % 6;
    constexpr int km = (ks + 16) % 18, abm = (ks + 2 + 2 * PAR) % 4;
    constexpr bool wr_prev = (sl == 1 && CEPI != 0) || (sl == 0 && WPIN);
    if constexpr (u == 0 && hf == 0) { if constexpr (YW && G == 0) WAITV(0); if constexpr (wr_prev) WL(1); else WL(0); CB(); BAR(); CB(); SB(); if constexpr (YW && G == 0) { st_flag(s.rws, pc.yfo, pc.yf, pc.l0); SB(); } }
    if constexpr (hf == 0 && u >= 2) { constexpr int n = rst(u) - 4 * (u - 2) - 5 + (LOADB ? 4 : 0); WL(n > 15 ? 15 : n); SB(); }
    if constexpr (ks >= 2 || HASPREV) {
      f32x16 c;
      if constexpr (ks == 2 && hf == 0) { if constexpr (INIT) c = binit; else for (int i = 0; i < 16; i++) c[i] = 0.f; } else c = s.acc;
      const bf16x8 a = cat(s.A[abm][2 * hf], s.A[abm][2 * hf + 1]);
      f32x16 d;
      if constexpr ((ks < 2 ? PORI : ORI) == 0) d = MFMA32(a, s.B[2 * km + hf], c); else d = MFMA32(s.B[2 * km + hf], a, c);
      if constexpr (ks == 1 && hf == 1) prev = d; else s.acc = d;
    }
    if constexpr (hf == 0) do_reads<rst(u), rst(u + 1), sl, PAR, IMG>(s);
    if constexpr (INIT && G == 1) {
#pragma unroll
      for (int m = 0; m < 4; m++) set4(binit, m, *(LAS f32x4*)(s.biasp + pc.boff + m * 32));
    }
    if constexpr (LOADB && hf == 1) { s.B[2 * ks] = *(LAS bf16x8*)(s.hr + (2 * ks) * 1024); s.B[2 * ks + 1] = *(LAS bf16x8*)(s.hr + (2 * ks + 1) * 1024); }
    if constexpr (CEPI != 0 && ks < 4) epi_step<CEPI, ks + 16, 16, hf>(prev, g, pc.cpm, pc.cone);
    if constexpr ((EPI == 1 || EPI == 2) && ks >= 2) epi_step<EPI, ks - 2, LOADB ? 5 : 16, hf>(prev, g, s.pm, s.one);
    if constexpr (CEPI != 0 && G == 10) frag_write<4>(g, s.lb + pc.w1c);
    if constexpr ((EPI == 1 || EPI == 2) && !LOADB && G == 34) frag_write<0>(g, s.lb + pc.w0);
    if constexpr (LOADB && G == 22) { frag_write<0>(g, s.lb + pc.w0); frag_write<4>(g, s.lb + pc.w0 + 1024); }
    if constexpr (EPI == 3 && G == 7) __builtin_amdgcn_raw_buffer_store_b128(pack8(prev, 0), s.rws, s.vy, pc.y0, SC1);
    if constexpr (EPI == 3 && G == 9) __builtin_amdgcn_raw_buffer_store_b128(pack8(prev, 1), s.rws, s.vy, pc.y1, SC1);
    SGB(0x8, 1); SGB(0x100, 8); SGB(0x200, 2); SGB(0x2, 24); SGB(0x40, 1);
    SB();
    do_gap<G + 1, PAR, IMG, ORI, PORI, INIT, EPI, CEPI, HASPREV, LOADB, YW, WPIN>(pc, s, g, prev, binit);
  }
}
#define PASS(PAR, IMG, ORI, PORI, INIT, EPI, CEPI, HASPREV, LOADB, YW, WPIN) do { STAMP(npass + 2); npass++; do_gap<0, PAR, IMG, ORI, PORI, INIT, EPI, CEPI, HASPREV, LOADB, YW, WPIN>(pc, s, g, prev, binit); } while (0)

DI int yoff(int b, int r, int j) {
  const int q = b / 9, b9 = b - 9 * q; const int hi = b9 >= 5; const int jo = 2 * q + hi, be = b9 - 5 * hi;
  return WS_YP + (((r * 8 + jo) * 8 + j) * 5 + be) * 2048;
}

DI int y_need(int j, int rot, int pi) {
  const int b0 = 9 * (j >> 1) + 5 * (j & 1);
  const int la = (b0 + 2 * pi) >> 2, lb = (b0 + 2 * pi + (pi < 2 ? 1 : 0)) >> 2;
  const int pa = la - rot + (la < rot ? 9 : 0), pb = lb - rot + (lb < rot ? 9 : 0);
  return (pi == 2 && (j & 1)) ? 0 : (pa > pb ? pa : pb) + 1;
}
DI int need_mask(int j, int rot, int lo) { return (y_need(j, rot, 0) >= lo ? 1 : 0) | (y_need(j, rot, 1) >= lo ? 2 : 0) | (y_need(j, rot, 2) >= lo ? 4 : 0); }
constexpr int YPRE = 24, YLATE = 0;
DI void y_issue(u32x4 (&yv)[3][8], rsrc_t rws, int mask, int vy, int ybase) {
#pragma unroll
  for (int pi = 0; pi < 3; pi++) { const int vo = ((mask >> pi) & 1) && (pi < 2 || vy < 2048) ? vy : OOB;
#pragma unroll
    for (int i = 0; i < 8; i++) aload16(yv[pi][i], rws, vo, ybase + i * 10240 + pi * 4096); }
}
DI void y_gate(u32x4 (&yv)[8], u32x2 b0, u32x2 b1, float (&g)[8]) {
  unsigned klo = 0x00003f80u, khi = 0x3f800000u; asm("" : "+s"(klo), "+s"(khi));
#pragma unroll
  for (int e = 0; e < 8; e++) g[e] = 0.f;
#pragma unroll
  for (int i = 0; i < 8; i++) { PIN(yv[i]);
#pragma unroll
    for (int e = 0; e < 4; e++) { const unsigned w = yv[i][e]; const bf16x2 v = __builtin_bit_cast(bf16x2, w);
      g[2 * e] = __builtin_amdgcn_fdot2_f32_bf16(v, __builtin_bit_cast(bf16x2, klo), g[2 * e], false);
      g[2 * e + 1] = __builtin_amdgcn_fdot2_f32_bf16(v, __builtin_bit_cast(bf16x2, khi), g[2 * e + 1], false); } }
#pragma unroll
  for (int e = 0; e < 4; e++) {
    auto sw = __builtin_amdgcn_permlane32_swap(__builtin_bit_cast(unsigned, g[e]), __builtin_bit_cast(unsigned, g[4 + e]), false, false);
    g[e] = __builtin_bit_cast(float, (unsigned)sw[0]); g[4 + e] = __builtin_bit_cast(float, (unsigned)sw[1]);
  }
#pragma unroll
  for (int e = 0; e < 8; e++) { const unsigned bw = (e < 4 ? b0 : b1)[(e >> 1) & 1];
    const float y = g[e] + ((e & 1) ? bfhi(bw) : bflo(bw));
    g[e] = y * __builtin_amdgcn_rcpf(1.0f + __builtin_amdgcn_exp2f(-1.44269504089f * y)); }
}

struct SS { LAS char* a[5]; LAS char* b; s16x4 A[2][5][2]; bf16x8 B[2]; f32x4 acc[5]; };
template <int SG> DI void s_steps(SS& q, rsrc_t rws, const PC& pc, u32x4 (&yv)[3][8], int mid, int vy, int ybase) {
  if constexpr (SG <= 18) {
    if constexpr (SG < 18 && SG % 3 == 0) {
      if constexpr (SG == 6) asm volatile("s_waitcnt vmcnt(%0)" ::"i"(YPRE) : "memory");
      WL(0); CB(); BAR(); CB(); SB();
      if constexpr (SG == 6) { st_flag(rws, pc.yfo, pc.yf, pc.l0); SB(); }
      if constexpr (SG == 0) { y_issue(yv, rws, mid, vy, ybase); SB(); }
    }
    if constexpr (SG >= 1) {
#pragma unroll
      for (int n = 0; n < 5; n++) {
        f32x4 c; if constexpr (SG == 1) c = f32x4{0.f, 0.f, 0.f, 0.f}; else c = q.acc[n];
        q.acc[n] = MFMA16(cat(q.A[(SG - 1) & 1][n][0], q.A[(SG - 1) & 1][n][1]), q.B[(SG - 1) & 1], c);
      }
    }
    if constexpr (SG < 18) {
      constexpr int o = (SG % 9) * 8192 - (SG % 9 >= 6 ? 49152 : 0);
      q.B[SG & 1] = *(LAS bf16x8*)(q.b + SG * 1024);
#pragma unroll
      for (int n = 0; n < 5; n++) { q.A[SG & 1][n][0] = trd(q.a[n], o); q.A[SG & 1][n][1] = trd(q.a[n], o + 1024); }
      if constexpr (SG % 9 == 5) { for (int n = 0; n < 5; n++) q.a[n] += 49152; }
      if constexpr (SG % 9 == 8) { for (int n = 0; n < 5; n++) q.a[n] -= 49152; }
      SGB(0x8, 1); SGB(0x100, 3); SGB(0x8, 1); SGB(0x100, 2); SGB(0x8, 1); SGB(0x100, 2); SGB(0x8, 1); SGB(0x100, 2); SGB(0x8, 1); SGB(0x100, 2);
    }
    SB();
    s_steps<SG + 1>(q, rws, pc, yv, mid, vy, ybase);
  }
}

DI void consumer(const Params& p, LAS char* smem, int wid, int lane, int r, int j, int rot, u64 tag, unsigned stamp_) {
  int npass = 0; (void)npass; STAMPC(40);
  CS s; GS g; f32x16 prev, binit; PC pc;
  const int th = wid & 1, cw = wid >> 1;
  const int gq = lane >> 4, q = (lane >> 2) & 3, pp = lane & 3, h = lane >> 5, g2 = gq & 1;
  {
    const int c = 4 * cw + 2 * g2 + (pp >> 1);
    s.an[0] = smem + RING_OFF + h * 1024 + q * 128 + ((c ^ (((q >> 1) & 1) << 2)) * 16) + (pp & 1) * 8;
    s.ap[0] = smem + RING_OFF + (4 * h + q) * 128 + ((c ^ (((q >> 1) & 1) << 2)) * 16) + (pp & 1) * 8;
    s.an[1] = s.an[0] + 2 * SLOT_B; s.ap[1] = s.ap[0] + 2 * SLOT_B; PIN(s.an[1]); PIN(s.ap[1]);
    s.lb = smem + lane * 16; s.hr = s.lb + th * 36864; s.biasp = smem + BIAS_OFF + cw * 128 + h * 16;
    s.rws = mkrs(p.ws, WS_END);
    s.vy = (2 * th + g2) * 512 + h * 256 + (lane & 15) * 16;
    s.pm = true; s.one = false;
  }
#pragma unroll
  for (int k = 0; k < 36; k++) s.B[k] = *(LAS bf16x8*)(smem + th * 36864 + k * 1024 + ((lane ^ (2 * (k & 3) + h)) * 16));
  const int ytab = yoff(lane < 36 ? lane : 0, r, j);
#define YOFF(L) do { const int b_ = 4 * (L) + 2 * cw; pc.y0 = __builtin_amdgcn_readlane(ytab, b_); pc.y1 = __builtin_amdgcn_readlane(ytab, b_ + 1); } while (0)
  const int hb = th * 36864 + cw * 2048;
  const int kb = th * 36864 + 24576;
  const int qb = PQ_OFF + th * 6144;
  pc.cpm = true; pc.cone = false; pc.y0 = pc.y1 = 0; pc.yf = tag; pc.yfo = WS_FY + (r * 8 + j) * 8; pc.l0 = wid == 0 && lane == 0;
  pc.boff = wrap9(rot) * 256; pc.w0 = 0; pc.w1c = 0;                       PASS(0, 0, 0, 0, 1, 0, 0, 0, 0, 0, 0);
  pc.boff = wrap9(1 + rot) * 256; pc.w0 = hb;                              PASS(1, 0, 0, 0, 1, 1, 0, 1, 0, 0, 0);
  pc.boff = wrap9(2 + rot) * 256; pc.w0 = hb + 4096; pc.w1c = hb + 1024;   PASS(0, 0, 0, 0, 1, 1, 1, 1, 0, 0, 1);
  pc.boff = wrap9(3 + rot) * 256; pc.w1c = pc.w0 + 1024; pc.w0 = hb + 8192; PASS(1, 0, 0, 0, 1, 1, 1, 1, 0, 0, 1);
  pc.w1c = pc.w0 + 1024; pc.w0 = hb + 3 * 4096;                            PASS(0, 0, 1, 0, 0, 1, 1, 1, 0, 0, 1);
  s.pm = (cw == 0); pc.w1c = pc.w0 + 1024; pc.w0 = kb + cw * 2048;         PASS(1, 0, 1, 1, 0, 2, 1, 1, 0, 0, 1);
  pc.cpm = s.pm; pc.w1c = pc.w0 + 1024; pc.w0 = kb + 4096 + cw * 2048;     PASS(0, 0, 1, 1, 0, 2, 2, 1, 0, 0, 1);
  pc.w1c = pc.w0 + 1024; pc.w0 = cw == 0 ? qb + 4096 : DUMMY_OFF;
  s.pm = ((lane + 8) & 31) < 16; s.one = (lane & 31) == 16;                PASS(1, 0, 0, 1, 0, 2, 2, 1, 0, 0, 1);
  pc.cpm = s.pm; pc.cone = s.one; pc.w1c = cw == 0 ? pc.w0 + 1024 : DUMMY_OFF; pc.w0 = qb + cw * 2048; s.pm = true; s.one = false;
  pc.boff = wrap9(4 + rot) * 256;                                          PASS(0, 0, 0, 0, 1, 2, 2, 1, 0, 0, 1);
  pc.cpm = true; pc.cone = false; pc.w1c = pc.w0 + 1024; pc.w0 = hb + 4 * 4096;
  pc.boff = wrap9(5 + rot) * 256;                                          PASS(1, 0, 0, 0, 1, 1, 2, 1, 0, 0, 1);
  pc.w1c = pc.w0 + 1024; pc.w0 = hb + 5 * 4096; pc.boff = wrap9(6 + rot) * 256;  PASS(0, 0, 0, 0, 1, 1, 1, 1, 0, 0, 1);
  pc.w1c = pc.w0 + 1024; pc.w0 = hb + 6 * 4096; pc.boff = wrap9(7 + rot) * 256;  PASS(1, 0, 0, 0, 1, 1, 1, 1, 0, 0, 1);
  pc.w1c = pc.w0 + 1024; pc.w0 = hb + 7 * 4096; pc.boff = wrap9(8 + rot) * 256;  PASS(0, 0, 0, 0, 1, 1, 1, 1, 0, 0, 1);
  pc.w1c = pc.w0 + 1024; pc.w0 = hb + 8 * 4096;                            PASS(1, 1, 0, 0, 0, 1, 1, 1, 1, 0, 1);
  for (int i = 1; i < 9; i += 2) {
    YOFF(wrap9(i - 1 + rot));   PASS(0, 1, 0, 0, 0, 3, 0, 1, 0, 0, 0);
    YOFF(wrap9(i + rot)); pc.yf = tag + i;   PASS(1, 1, 0, 0, 0, 3, 0, 1, 0, 1, 0);
  }
  STAMP(24);
  {
    f32x16 d = MFMA32(cat(s.A[2][0], s.A[2][1]), s.B[32], s.acc); d = MFMA32(cat(s.A[2][2], s.A[2][3]), s.B[33], d);
    d = MFMA32(cat(s.A[3][0], s.A[3][1]), s.B[34], d); d = MFMA32(cat(s.A[3][2], s.A[3][3]), s.B[35], d);
    YOFF(wrap9(8 + rot));
    __builtin_amdgcn_raw_buffer_store_b128(pack8(d, 0), s.rws, s.vy, pc.y0, SC1);
    __builtin_amdgcn_raw_buffer_store_b128(pack8(d, 1), s.rws, s.vy, pc.y1, SC1);
    SB();
  }
  SS sq;
  {
    const int rho = 4 * g2 + q; const int wo0 = (288 * (j >> 1) + (j & 1) * 160) & 127;
#pragma unroll
    for (int n = 0; n < 5; n++) { const int o = wo0 + 32 * n; const int lc = o >> 7, tau = (o >> 5) & 3;
      sq.a[n] = smem + RING_OFF + lc * 4096 + (gq >> 1) * 2048 + rho * 128 + (((2 * tau + (pp >> 1)) ^ (((rho >> 1) & 3) << 1)) * 16) + (pp & 1) * 8; }
    sq.b = s.lb + wid * 18432;
  }
  const int c0 = 288 * (j >> 1) + (j & 1) * 160, tok = r * 64 + wid * 16 + (lane & 15);
  u32x2 xv[5], bv[5];
  {
    const rsrc_t rx = mkrs(p.x, 2048 * 1152), rb2 = mkrs(p.b2, 1152);
#pragma unroll
    for (int n = 0; n < 5; n++) { aload8p(xv[n], rx, tok * 1152 + gq * 8, c0 + 32 * n); aload8p(bv[n], rb2, gq * 8, c0 + 32 * n); }
  }
  pc.yf = tag + 9;
  const int own = need_mask(j, rot, 1), late = need_mask(j, rot, 8);
  const int vy = (gq >> 1) * 2048 + wid * 512 + g2 * 256 + (lane & 15) * 16, ybase = WS_YP + (r * 8 + j) * 81920;
  u32x4 yv[3][8];
  s_steps<0>(sq, s.rws, pc, yv, own & ~late, vy, ybase);
  STAMP(25);
  float gt[3][8];
#pragma unroll
  for (int e = 0; e < 8; e++) gt[2][e] = 0.f;
  WAITV(YLATE);
#pragma unroll
  for (int n = 0; n < 5; n++) PIN(bv[n]);
#pragma unroll
  for (int pi = 0; pi < 3; pi++) if (((own & ~late) >> pi) & 1) y_gate(yv[pi], bv[2 * pi], bv[pi < 2 ? 2 * pi + 1 : 4], gt[pi]);
  if (late) {
    poll_block(s.rws, (lane & 7) * 8, WS_FY + r * 64, tag, 9);
    STAMP(26);
    y_issue(yv, s.rws, late, vy, ybase);
    WAITV(0);
#pragma unroll
    for (int pi = 0; pi < 3; pi++) if ((late >> pi) & 1) y_gate(yv[pi], bv[2 * pi], bv[pi < 2 ? 2 * pi + 1 : 4], gt[pi]);
  }
  STAMP(27);
  const rsrc_t rout = mkrs(p.out, 2048 * 1152);
#pragma unroll
  for (int n = 0; n < 5; n++) {
    PIN(xv[n]);
    const float* gv = &gt[n >> 1][4 * (n & 1)];
    float o[4];
#pragma unroll
    for (int e = 0; e < 4; e++) { const unsigned xw = xv[n][e >> 1]; o[e] = __builtin_fmaf(sq.acc[n][e], gv[e], (e & 1) ? bfhi(xw) : bflo(xw)); }
    __builtin_amdgcn_raw_buffer_store_b64(u32x2{pk2(o[0], o[1]), pk2(o[2], o[3])}, rout, (n < 4 || (j & 1) == 0) ? tok * 1152 + gq * 8 : OOB, c0 + 32 * n, 0);
  }
  STAMP(28);
  STAMPC(41); if (wid == 0) STAMP_OUT(0);
}

constexpr int NA = NG1A;
constexpr int SQ0 = 3 * NA, SQ1 = SQ0 + 12;
constexpr int OS = 55;
struct LS { rsrc_t r1, r2, rw, ro; int vo1, vo2, voq, voq2, voo, voo2; unsigned d, sm; int j, r, rot, line0, k; };
DI int g1_so(const LS& c, int pi) { return c.j * 1152 + wrap9(pi + c.rot) * 128 + c.k * 64 * 9216; }
#define FIRE(E, N, AG, RS, LDS, VO, SO, STEP) do { const unsigned l_ = (LDS); const int s_ = (SO); asm volatile("" ::"s"(l_), "s"(s_)); \
    WAITV(8 + (E)); BAR(); CB(); dma_run<N, AG>(RS, l_, VO, s_, STEP); } while (0)
DI void ring_loader(const LS& c) {
  __builtin_amdgcn_s_setprio(RLPRIO);
  FIRE(0, 8, 0, c.r1, c.d + 2 * SLOT_B, c.vo1, g1_so(c, 0) + 2 * 192 * 9216, 8 * 9216);
  for (int pi = 1; pi < NA; pi++) { const int so = g1_so(c, pi);
#pragma unroll
    for (int t = 0; t < 3; t++) FIRE(0, 8, 0, c.r1, c.d + t * SLOT_B, c.vo1, so + t * 192 * 9216, 8 * 9216); }
  for (int q = 0; q < 4; q++) { const int so = WS_WQP + c.j * WQP_J + q * 3 * SLOT_B + c.k * 8192; const int vo = q == 2 ? c.voq2 : c.voq;
#pragma unroll
    for (int t = 0; t < 3; t++) FIRE(0, 8, 1, c.rw, c.d + t * SLOT_B, vo, so + t * SLOT_B, 1024); }
  for (int pi = NA; pi < 9; pi++) { const int so = g1_so(c, pi);
#pragma unroll
    for (int t = 0; t < 3; t++) FIRE(0, 8, 0, c.r1, c.d + t * SLOT_B, c.vo1, so + t * 192 * 9216, 8 * 9216); }
  int rb[3];
#pragma unroll
  for (int t = 0; t < 3; t++) rb[t] = (c.j * 576 + wrap9(wrap9(t * 3 + c.k) + c.rot) * 64) * 1152;
  for (int pi = 0; pi < 6; pi++) { const int co = wrap9(pi + c.rot) * 128;
#pragma unroll
    for (int t = 0; t < 3; t++) FIRE(0, 8, 0, c.r2, c.d + t * SLOT_B, c.vo2, rb[t] + co, 8 * 1152); }
  const unsigned od = c.sm + c.k * 24576; const int oo = WS_OF + c.r * 73728 + c.k * 24576;
#pragma unroll
  for (int n = 0; n < 9; n++) { const int co = wrap9(6 + n / 3 + c.rot) * 128;
    FIRE(n == 0 ? 0 : (n == 1 ? 3 : 6), 8, 0, c.r2, c.d + (n % 3) * SLOT_B, c.vo2, rb[n % 3] + co, 8 * 1152);
    if (n < 8) dma_run<3, 1>(c.rw, od + n * 3072, c.voq, oo + n * 3072, 1024); }
#pragma unroll
  for (int n = 0; n < 6; n++) { const int sg = n * 3 + c.k; const unsigned d = c.d + (n % 3) * SLOT_B;
    if (sg < 16) { const int so = (72 * (sg >> 1) + 32 * (sg & 1)) * 1152 + c.line0 * 128;
      FIRE(n == 0 ? 3 : 0, 4, 0, c.ro, d, c.voo, so, 8 * 1152); dma_run<4, 0>(c.ro, d + 4096, c.voo, so + 128, 8 * 1152);
    } else { const int so = 288 * (sg - 16) * 1152 + c.line0 * 128;
      FIRE(0, 2, 0, c.ro, d, c.voo2, so, 4 * 1152);
#pragma unroll
      for (int q = 1; q < 4; q++) dma_run<2, 0>(c.ro, d + q * 2048, c.voo2, so + (q >> 1) * 128 + (q & 1) * 144 * 1152, 4 * 1152);
    } }
  WAITV(8); BAR();
  WAITV(0); BAR();
}

DI void kv_pair(LAS char* lb, rsrc_t rws, int ta, int tb, int lane, int so) {
  const int e32 = lane & 31, h = lane >> 5;
  const int oa = ta == 4 ? PQ_OFF + 4096 : 24576 + ta * 2048, sa = ta == 4 ? 6144 : 36864;
  const int ob = tb == 4 ? PQ_OFF + 4096 : 24576 + tb * 2048, sb = tb == 4 ? 6144 : 36864;
  f32x16 d; for (int i = 0; i < 16; i++) d[i] = 0.f;
#pragma unroll
  for (int k = 0; k < 4; k++)
    d = MFMA32(*(LAS bf16x8*)(lb + oa + (k >> 1) * sa + (k & 1) * 1024), *(LAS bf16x8*)(lb + ob + (k >> 1) * sb + (k & 1) * 1024), d);
  const int e = tb == 4 ? (e32 == 16 ? 72 : (e32 >= 8 && e32 < 16 ? 56 + e32 : -1)) : (tb >> 1) * 32 + e32;
  const int hd = e / 18;
#pragma unroll
  for (int m = 0; m < 4; m++) { const int d0 = (ta >> 1) * 32 + 8 * m + 4 * h;
    const bool need = e >= 0 && d0 < 72 && (e == 72 || (d0 + 3 >= 18 * hd && d0 < 18 * hd + 18));
    __builtin_amdgcn_raw_buffer_store_b128(__builtin_bit_cast(u32x4, f32x4{d[4 * m], d[4 * m + 1], d[4 * m + 2], d[4 * m + 3]}), rws, need ? lane * 16 : OOB, so + m * 1024, SC1); }
}
DI bool vm_busy() { return (__builtin_amdgcn_s_getreg(7 | (31 << 11)) & 0x00C0000Fu) != 0; }
#define JOIN() do { CB(); BAR(); CB(); nb++; } while (0)
#define UNTIL(X) do { while (nb <= (X)) JOIN(); } while (0)
#define POLITE(LIM) do { while (vm_busy() && nb <= (LIM)) JOIN(); WAITV(0); } while (0)
#define SKIP(N, LIM) do { for (int k_ = 0; k_ < (N) && nb <= (LIM); k_++) JOIN(); } while (0)
#define POLL(VO, SO, NEED, FIRST, LIM) do { bool ok_ = false; SKIP(FIRST, LIM); \
    while (nb <= (LIM)) { u32x2 f_; aload8(f_, rws, VO, SO); JOIN(); POLITE(LIM); PIN(f_); if (tag_ok(f_, tag, NEED)) { ok_ = true; break; } SKIP(1, LIM); } \
    if (!ok_) poll_block(rws, VO, SO, tag, NEED); } while (0)

DI void o_half(LAS char* smem, rsrc_t rws, const u32x4 (&a)[12], int th, int lane, int r, int j) {
  const int h = lane >> 5;
  bf16x8 b[5];
#pragma unroll
  for (int i = 0; i < 4; i++) b[i] = *(LAS bf16x8*)(smem + PQ_OFF + th * 6144 + i * 1024 + lane * 16);
  b[4] = cat(trd(smem + PQ_OFF + th * 6144 + 4096 + ((lane >> 4) & 1) * 1024 + (24 + 4 * h + ((lane >> 2) & 3) + 32 * (lane & 1)) * 16 + ((lane >> 1) & 1) * 8, 0), s16x4{0, 0, 0, 0});
  f32x16 d2, de[2]; for (int i = 0; i < 16; i++) { d2[i] = 0.f; de[0][i] = 0.f; de[1][i] = 0.f; }
#pragma unroll
  for (int i = 0; i < 5; i++) d2 = MFMA32(__builtin_bit_cast(bf16x8, a[7 + i]), b[i], d2);
#pragma unroll
  for (int i = 0; i < 3; i++) de[0] = MFMA32(__builtin_bit_cast(bf16x8, a[i]), b[i], de[0]);
#pragma unroll
  for (int i = 0; i < 4; i++) de[1] = MFMA32(__builtin_bit_cast(bf16x8, a[3 + i]), b[i + 1], de[1]);
  float rd[4];
#pragma unroll
  for (int i = 0; i < 4; i++) rd[i] = __builtin_amdgcn_rcpf(d2[4 + i] + 1e-6f);
  const int ob = WS_OF + (r * 4 + 2 * th) * 18432;
  const int vo = ((lane >> 4) & 1) * 18432 + ((lane & 15) + 16 * h) * 16;
#pragma unroll
  for (int cc = 0; cc < 2; cc++) {
#pragma unroll
    for (int e = 0; e < 16; e++) { const int e0 = cc * 32 + (e & 3) + 8 * (e >> 2);
      const float r0 = rd[e0 / 18], r1 = rd[(e0 + 4) / 18]; de[cc][e] *= (e0 / 18 == (e0 + 4) / 18) ? r0 : (h ? r1 : r0); }
    __builtin_amdgcn_raw_buffer_store_b128(pack8(de[cc], 0), rws, vo, ob + (2 * j + cc) * 1024, SC1);
    __builtin_amdgcn_raw_buffer_store_b128(pack8(de[cc], 1), rws, vo, ob + (2 * j + cc) * 1024 + 512, SC1);
  }
  const int vo2 = ((lane >> 4) & 1) * 18432 + ((lane & 15) + 16 * (j & 3)) * 16 + 8 * h;
  __builtin_amdgcn_raw_buffer_store_b64(u32x2{pk2(d2[0] * rd[3], d2[1] * rd[3]), pk2(d2[2] * rd[3], d2[3] * rd[3])}, rws, vo2, ob + (16 + (j >> 2)) * 1024, SC1);
}

DI void service(const Params& p, LAS char* smem, rsrc_t rws, int lane, int r, int j, int rot, u64 tag, const u32x4 (&rp)[9], const int (&rpo)[9], unsigned stamp_) {
  const bool l0 = lane == 0;
  int nb = 0;
  UNTIL(0);
#pragma unroll
  for (int m = 0; m < 9; m++) __builtin_amdgcn_raw_buffer_store_b128(rp[m], rws, rpo[m], WS_WQP + j * WQP_J, SC1);
  JOIN(); POLITE(SQ0 - 3);
  st_flag(rws, WS_FREP + (j * 32 + r) * 8, tag, l0);
  UNTIL(SQ0 - 7); POLL((lane & 31) * 8, WS_FREP + j * 256, 0, 0, SQ0 - 3);
  STAMP(11);
  UNTIL(SQ1 + 2);
  __builtin_amdgcn_s_setprio(SVPRIO);
  STAMP(2);
  {
    const int kvo = WS_KVP + (j * 32 + r) * KVP_WG; LAS char* lb = smem + lane * 16;
    kv_pair(lb, rws, 0, 1, lane, kvo); kv_pair(lb, rws, 0, 3, lane, kvo + 4096); STAMP(3); JOIN();
    kv_pair(lb, rws, 2, 1, lane, kvo + 8192); kv_pair(lb, rws, 2, 3, lane, kvo + 12288); JOIN();
    kv_pair(lb, rws, 2, 4, lane, kvo + 16384); kv_pair(lb, rws, 0, 4, lane, kvo + 20480); JOIN();
    kv_pair(lb, rws, 4, 3, lane, kvo + 24576); kv_pair(lb, rws, 4, 4, lane, kvo + 28672);
  }
  constexpr int DL = OS - 1;
  SKIP(1, DL); POLITE(DL);
  st_flag(rws, WS_FKV1 + (j * 32 + r) * 8, tag, l0);
  STAMP(4);
  POLL((lane & 31) * 8, WS_FKV1 + j * 256, 0, 2, DL);
  STAMP(5);
  {
    const int P = r * 48 + (lane < 48 ? lane : 47);
    const int f = P >> 7, l2 = (P & 127) >> 1, half = P & 1, rho = l2 & 31, hh = l2 >> 5;
    const int T = f < 3 ? 0 : (f < 7 ? 1 : 2), ks = f < 3 ? f : (f < 7 ? f - 2 : f - 7);
    const int d0 = 16 * ks + 8 * half + 4 * hh;
    const int hd = T < 2 ? (32 * T + rho) / 18 : (rho < 8 ? 3 : (rho < 16 ? (rho & 3) : -9));
    const bool need = lane < 48 && d0 + 3 >= 18 * hd && d0 < 18 * hd + 18;
    const int ta = d0 >> 5;
    const int pair = T < 2 ? (ta == 2 ? 6 : 2 * ta + T) : (ta == 0 ? 5 : (ta == 1 ? 4 : 7));
    const int el = T < 2 ? rho : (rho < 8 ? 8 + rho : 16);
    const int vo = need ? pair * 4096 + ((d0 & 31) >> 3) * 1024 + (el + 32 * hh) * 16 : OOB;
    u32x4 v[32];
#pragma unroll
    for (int i = 0; i < 32; i++) aload16(v[i], rws, vo, WS_KVP + (j * 32 + i) * KVP_WG);
    SKIP(1, DL); POLITE(DL);
    float a[4];
#pragma unroll
    for (int i = 0; i < 32; i++) { PIN(v[i]);
#pragma unroll
      for (int e = 0; e < 4; e++) { const unsigned w = v[i][e]; const float fv = __builtin_bit_cast(float, w); a[e] = i == 0 ? fv : a[e] + fv; } }
#pragma unroll
    for (int e = 0; e < 4; e++) { const int d = d0 + e; a[e] = (d >= 18 * hd && d < 18 * hd + 18) ? a[e] : 0.f; }
    __builtin_amdgcn_raw_buffer_store_b64(u32x2{pk2(a[0], a[1]), pk2(a[2], a[3])}, rws, lane < 48 ? P * 8 : OOB, WS_KVF + j * 12288, SC1);
  }
  SKIP(1, DL); POLITE(DL);
  st_flag(rws, WS_FKV2 + (j * 32 + r) * 8, tag, l0);
  STAMP(6);
  POLL((lane & 31) * 8, WS_FKV2 + j * 256, 0, 2, DL);
  STAMP(7);
  {
    u32x4 a[12];
#pragma unroll
    for (int i = 0; i < 12; i++) aload16(a[i], rws, lane * 16, WS_KVF + j * 12288 + i * 1024);
    SKIP(1, DL); POLITE(DL);
    STAMP(8);
#pragma unroll
    for (int i = 0; i < 12; i++) PIN(a[i]);
    o_half(smem, rws, a, 0, lane, r, j); SKIP(1, DL);
    o_half(smem, rws, a, 1, lane, r, j);
  }
  STAMP(9);
  SKIP(1, DL); POLITE(DL);
  st_flag(rws, WS_FO + (r * 8 + j) * 8, tag, l0);
  POLL((lane & 7) * 8, WS_FO + r * 64, 0, 2, DL);
  STAMP(13);
  if (r == 0 && j == 0) st_flag(rws, WS_EPOCH, tag, l0);
  UNTIL(63);
  POLL((lane & 7) * 8, WS_FY + r * 64, 7, 0, 65);
  STAMP(14);
  UNTIL(71);
  STAMP_OUT(1);
}

__global__ __launch_bounds__(512, 2) NOPK void fused_main(Params p) {
  extern __shared__ __attribute__((aligned(16))) char smem_[];
  LAS char* smem = (LAS char*)smem_; const unsigned sm = (unsigned)(size_t)smem;
  const int tid = threadIdx.x, lane = tid & 63; const int wid = __builtin_amdgcn_readfirstlane(tid >> 6);
  const int j = (blockIdx.x & 6) | ((blockIdx.x >> 3) & 1), r = ((blockIdx.x >> 4) << 1) | (blockIdx.x & 1);
  const int rot = r % 9;
  unsigned stamp_ = 0; STAMP(0);
  const int tau = lane >> 3, li = lane & 7;
  const rsrc_t rx = mkrs(p.x, 2048 * 1152);
  u32x4 xv[9];
#pragma unroll
  for (int m = 0; m < 9; m++) xv[m] = __builtin_amdgcn_raw_buffer_load_b128(rx, (r * 64 + wid * 8 + tau) * 1152 + li * 16, m * 128, 0);
  LS c;
  c.rw = mkrs(p.ws, WS_END);
  u32x4 rp[9]; int rpo[9];
  if (wid == 7) {
    const rsrc_t rq = mkrs(p.wqkv, 576 * 3456);
#pragma unroll
    for (int m = 0; m < 9; m++) {
      const int idx = m * 64 + lane; const int row = 18 * r + (idx >> 5), ps = (idx >> 3) & 3, cp = idx & 7;
      const int cs = cp ^ (((row >> 1) & 1) << 2), cw = cs >> 2, c4 = cs & 3;
      const int col = ps < 2 ? (cw ? 1152 : 576) + 32 * ps + 8 * c4 : (ps == 3 ? 32 * cw + 8 * c4 : (c4 == 0 ? 576 : (c4 == 1 ? 1152 : 0)) + 64);
      const bool on = (ps != 2 || (cw == 0 && c4 != 2));
      rp[m] = __builtin_amdgcn_raw_buffer_load_b128(rq, on ? row * 3456 + (72 * j + col) * 2 : OOB, 0, 0);
      rpo[m] = on ? (ps * 18 + (row >> 5)) * 4096 + (row & 31) * 128 + cp * 16 : OOB;
    }
  }
  const u32x2 ep = __builtin_amdgcn_raw_buffer_load_b64(c.rw, 0, WS_EPOCH, SC1);
  const float bv0 = bf2f(p.b1[j * 576 + tid]); const float bv1 = bf2f(p.b1[j * 576 + 512 + (tid & 63)]);
  {
    const int c1 = li ^ (((tau >> 1) & 1) << 2), c2 = li ^ (((tau >> 1) & 3) << 1);
    c.r1 = mkrs(p.w1, 576 * 9216); c.r2 = mkrs(p.w2, 4608 * 1152); c.ro = mkrs(p.wo, 576 * 1152);
    c.vo1 = tau * 9216 + c1 * 16; c.vo2 = tau * 1152 + c1 * 16; c.voq = lane * 16; c.voq2 = (c1 < 4 && c1 != 2) ? c.voq : OOB;
    c.voo = tau * 1152 + c2 * 16;
    c.voo2 = (72 * (tau >> 2) + 64 + (tau & 3)) * 1152 + c2 * 16;
    c.k = wid - 4; c.sm = sm; c.d = sm + RING_OFF + c.k * 8192; c.j = j; c.r = r; c.rot = rot; c.line0 = (288 * (j >> 1) + (j & 1) * 160) >> 7;
    if (wid >= 4 && wid < 7) { dma_run<8, 0>(c.r1, c.d, c.vo1, g1_so(c, 0), 8 * 9216); dma_run<8, 0>(c.r1, c.d + SLOT_B, c.vo1, g1_so(c, 0) + 192 * 9216, 8 * 9216); }
  }
  {
    float sum = 0.f, sq = 0.f;
#pragma unroll
    for (int m = 0; m < 9; m++)
#pragma unroll
      for (int e = 0; e < 4; e++) { const unsigned w = xv[m][e]; const bf16x2 v = __builtin_bit_cast(bf16x2, w);
        sum = __builtin_amdgcn_fdot2_f32_bf16(v, __builtin_bit_cast(bf16x2, 0x3f803f80u), sum, false); sq = __builtin_amdgcn_fdot2_f32_bf16(v, v, sq, false); }
#define RED8(x) do { x += __builtin_bit_cast(float, __builtin_amdgcn_update_dpp(0, __builtin_bit_cast(int, x), 0xB1, 0xf, 0xf, true)); \
                     x += __builtin_bit_cast(float, __builtin_amdgcn_update_dpp(0, __builtin_bit_cast(int, x), 0x4E, 0xf, 0xf, true)); \
                     x += __builtin_bit_cast(float, __builtin_amdgcn_update_dpp(0, __builtin_bit_cast(int, x), 0x141, 0xf, 0xf, true)); } while (0)
    RED8(sum); RED8(sq);
    const float mean = sum * (1.0f / 576.0f);
    const float var = __builtin_fmaxf(__builtin_fmaf(-mean, mean, sq * (1.0f / 576.0f)), 0.f);
    const float rstd = __builtin_amdgcn_rsqf(var + 1e-5f), mb = -mean * rstd;
#pragma unroll
    for (int m = 0; m < 9; m++)
#pragma unroll
      for (int e = 0; e < 4; e++) { const unsigned w = xv[m][e]; xv[m][e] = pk2(__builtin_fmaf(bflo(w), rstd, mb), __builtin_fmaf(bfhi(w), rstd, mb)); }
#pragma unroll
    for (int m = 0; m < 9; m++) PIN(xv[m]);
    STAMP(30);
    WAITV(0); BAR(); CB(); STAMP(31);
    const int tk = wid * 8 + tau;
    LAS char* wb = smem + (tk >> 5) * 36864 + (li >> 1) * 1024 + ((((tk & 31) + 32 * (li & 1)) ^ li) * 16);
#pragma unroll
    for (int m = 0; m < 9; m++) *(LAS u32x4*)(wb + m * 4096) = xv[m];
    *(LAS float*)(smem + BIAS_OFF + tid * 4) = bv0; if (tid < 64) *(LAS float*)(smem + BIAS_OFF + 2048 + tid * 4) = bv1;
  }
  const u64 tag = (((u64)(unsigned)__builtin_amdgcn_readfirstlane(ep[1]) << 32) | (unsigned)__builtin_amdgcn_readfirstlane(ep[0])) + 16;
  WL(0); CB(); BAR(); CB();
  STAMP(1);
  if (wid < 4) consumer(p, smem, wid, lane, r, j, rot, tag, stamp_); else if (wid < 7) ring_loader(c); else service(p, smem, c.rw, lane, r, j, rot, tag, rp, rpo, stamp_);
}

extern "C" void kernel_launch(void* const* d_in, const int* in_sizes, int n_in, void* d_out, int out_size, void* d_ws, size_t ws_size, hipStream_t stream) {
  Params p;
  p.x = (const u16*)d_in[0]; p.wqkv = (const u16*)d_in[1]; p.wo = (const u16*)d_in[2]; p.w1 = (const u16*)d_in[4]; p.b1 = (const u16*)d_in[5];
  p.w2 = (const u16*)d_in[6]; p.b2 = (const u16*)d_in[7]; p.out = (u16*)d_out; p.ws = (char*)d_ws;
  (void)hipFuncSetAttribute((const void*)fused_main, hipFuncAttributeMaxDynamicSharedMemorySize, LDS_BYTES);
  fused_main<<<256, 512, LDS_BYTES, stream>>>(p);
}
```

```cpp
#include <hip/hip_runtime.h>
#include <hip/hip_bf16.h>

typedef unsigned short u16;
typedef unsigned long long u64;
typedef short s16x4 __attribute__((ext_vector_type(4)));
typedef __bf16 bf16x8 __attribute__((ext_vector_type(8)));
typedef __bf16 bf16x2 __attribute__((ext_vector_type(2)));
typedef float f32x2 __attribute__((ext_vector_type(2)));
typedef float f32x4 __attribute__((ext_vector_type(4)));
typedef float f32x16 __attribute__((ext_vector_type(16)));
typedef unsigned u32x2 __attribute__((ext_vector_type(2)));
typedef unsigned u32x4 __attribute__((ext_vector_type(4)));
typedef __amdgpu_buffer_rsrc_t rsrc_t;
#define LAS __attribute__((address_space(3)))
#if defined(__HIP_DEVICE_COMPILE__)
#define NOPK __attribute__((target("no-packed-fp32-ops")))
#else
#define NOPK
#endif
#define DI __device__ __forceinline__ NOPK
#define BAR() __builtin_amdgcn_s_barrier()
#define SB() __builtin_amdgcn_sched_barrier(0)
#define CB() asm volatile("" ::: "memory")
#define SGB(m, n) __builtin_amdgcn_sched_group_barrier(m, n, 0)
#define WAITV(n) asm volatile("s_waitcnt vmcnt(%0)" ::"i"(n) : "memory")
#define WL(n) __builtin_amdgcn_s_waitcnt(0xC07F | ((n) << 8))
#define MFMA32(a, b, c) __builtin_amdgcn_mfma_f32_32x32x16_bf16(a, b, c, 0, 0, 0)
#define MFMA16(a, b, c) __builtin_amdgcn_mfma_f32_16x16x32_bf16(a, b, c, 0, 0, 0)
#define SC1 16

constexpr unsigned WS_EPOCH = 0;
constexpr unsigned WS_FKV1 = 4096;
constexpr unsigned WS_FKV2 = WS_FKV1 + 4096;
constexpr unsigned WS_FREP = WS_FKV2 + 4096;
constexpr unsigned WS_FO = WS_FREP + 4096;
constexpr unsigned WS_FY = WS_FO + 4096;
constexpr unsigned WS_WQP = 32768;
constexpr unsigned WQP_J = 4 * 18 * 4096;
constexpr unsigned WS_KVP = WS_WQP + 8 * WQP_J;
constexpr unsigned KVP_WG = 32768;
constexpr unsigned WS_KVF = WS_KVP + 256 * KVP_WG;
constexpr unsigned WS_OF = WS_KVF + 8 * 12288;
constexpr unsigned WS_YP = WS_OF + 32 * 73728;
constexpr unsigned WS_END = WS_YP + 32 * 8 * 8 * 10240;
#define MAPMODE 1
#define SVPRIO 2
#define NG1A 4
#define CODEPF 56
#define RLPRIO 0
constexpr int RING_OFF = 73728, SLOT_B = 24576, BIAS_OFF = 147456, PQ_OFF = 149760, DUMMY_OFF = 162048, LDS_BYTES = 163840;

DI float bf2f(u16 v) { return __builtin_bit_cast(float, ((unsigned)v) << 16); }
DI float bflo(unsigned v) { return __builtin_bit_cast(float, v << 16); }
DI float bfhi(unsigned v) { return __builtin_bit_cast(float, v & 0xffff0000u); }
DI unsigned pk2(float a, float b) { f32x2 v = {a, b}; bf16x2 r = __builtin_convertvector(v, bf16x2); return __builtin_bit_cast(unsigned, r); }
DI int wrap9(int v) { return v >= 9 ? v - 9 : v; }
DI rsrc_t mkrs(const void* p, unsigned bytes) { return __builtin_amdgcn_make_buffer_rsrc((void*)p, 0, bytes, 0x00020000); }
#define DMA_NEXT(POL) "s_add_u32 m0, m0, 0x400\n\ts_add_u32 %0, %0, %4\n\tbuffer_load_dwordx4 %2, %3, %0 offen " POL "lds\n\t"
#define DMA_FIRST(POL) "s_mov_b32 m0, %1\n\ts_nop 0\n\tbuffer_load_dwordx4 %2, %3, %0 offen " POL "lds\n\t"
template <int N, int AG> DI void dma_run(rsrc_t rs, unsigned lds, int vo, int so, int step) {
  if constexpr (N == 8 && !AG) asm volatile(DMA_FIRST("") DMA_NEXT("") DMA_NEXT("") DMA_NEXT("") DMA_NEXT("") DMA_NEXT("") DMA_NEXT("") DMA_NEXT("") : "+s"(so) : "s"(lds), "v"(vo), "s"(rs), "s"(step) : "memory", "scc");
  if constexpr (N == 8 && AG) asm volatile(DMA_FIRST("sc1 ") DMA_NEXT("sc1 ") DMA_NEXT("sc1 ") DMA_NEXT("sc1 ") DMA_NEXT("sc1 ") DMA_NEXT("sc1 ") DMA_NEXT("sc1 ") DMA_NEXT("sc1 ") : "+s"(so) : "s"(lds), "v"(vo), "s"(rs), "s"(step) : "memory", "scc");
  if constexpr (N == 4) asm volatile(DMA_FIRST("") DMA_NEXT("") DMA_NEXT("") DMA_NEXT("") : "+s"(so) : "s"(lds), "v"(vo), "s"(rs), "s"(step) : "memory", "scc");
  if constexpr (N == 3) asm volatile(DMA_FIRST("sc1 ") DMA_NEXT("sc1 ") DMA_NEXT("sc1 ") : "+s"(so) : "s"(lds), "v"(vo), "s"(rs), "s"(step) : "memory", "scc");
  if constexpr (N == 2) asm volatile(DMA_FIRST("") DMA_NEXT("") : "+s"(so) : "s"(lds), "v"(vo), "s"(rs), "s"(step) : "memory", "scc");
}
DI s16x4 trd(LAS char* p, int off) { return __builtin_amdgcn_ds_read_tr16_b64_v4i16((LAS s16x4*)(p + off)); }
DI bf16x8 cat(s16x4 a, s16x4 b) { return __builtin_bit_cast(bf16x8, __builtin_shufflevector(a, b, 0, 1, 2, 3, 4, 5, 6, 7)); }
DI void set4(f32x16& v, int i, f32x4 x) { v[4 * i] = x[0]; v[4 * i + 1] = x[1]; v[4 * i + 2] = x[2]; v[4 * i + 3] = x[3]; }
DI u32x4 pack8(const f32x16& p, int t) {
  return u32x4{pk2(p[8 * t], p[8 * t + 1]), pk2(p[8 * t + 2], p[8 * t + 3]), pk2(p[8 * t + 4], p[8 * t + 5]), pk2(p[8 * t + 6], p[8 * t + 7])};
}
DI void aload16(u32x4& d, rsrc_t rs, int vo, int so) { asm volatile("buffer_load_dwordx4 %0, %1, %2, %3 offen sc1" : "=v"(d) : "v"(vo), "s"(rs), "s"(so) : "memory"); }
DI void aload8(u32x2& d, rsrc_t rs, int vo, int so) { asm volatile("buffer_load_dwordx2 %0, %1, %2, %3 offen sc1" : "=v"(d) : "v"(vo), "s"(rs), "s"(so) : "memory"); }
DI void aload8p(u32x2& d, rsrc_t rs, int vo, int so) { asm volatile("buffer_load_dwordx2 %0, %1, %2, %3 offen" : "=v"(d) : "v"(vo), "s"(rs), "s"(so) : "memory"); }
constexpr int OOB = 0x7ffffff0;
#define PIN(x) asm volatile("" : "+v"(x))
DI bool tag_ok(u32x2 v, u64 tag, unsigned need) {
  const u64 d = (((u64)v[1] << 32) | v[0]) - tag;
  return __builtin_amdgcn_ballot_w64(d < need || d > 15) == 0;
}
DI void poll_block(rsrc_t rs, int vo, int so, u64 tag, unsigned need) {
  for (int it = 0; it < 20000; ++it) {
    u32x2 v; aload8(v, rs, vo, so); WAITV(0); PIN(v);
    if (tag_ok(v, tag, need)) break;
    __builtin_amdgcn_s_sleep(2);
  }
}
DI void st_flag(rsrc_t rs, int so, u64 v, bool on) { __builtin_amdgcn_raw_buffer_store_b64(u32x2{(unsigned)v, (unsigned)(v >> 32)}, rs, on ? 0 : OOB, so, SC1); }

#define STAMP(i)
#define STAMPC(i)
#define STAMP_OUT(w)
struct Params { const u16 *x, *wqkv, *wo, *w1, *b1, *w2, *b2; u16* out; char* ws; };

struct CS {
  bf16x8 B[36]; s16x4 A[4][4]; f32x16 acc;
  LAS char* an[2];
  LAS char* ap[2];
  LAS char* lb;
  LAS char* hr;
  LAS char* biasp;
  rsrc_t rws; int vy;
  bool pm, one;
};
struct GS { float x[16]; float a[16]; unsigned pk[8]; };
template <int EPI, int E, int ST> DI void epi_op(const f32x16& prev, GS& g, bool pm, bool one) {
  if constexpr (EPI == 1) {
    if constexpr (ST == 0) { float x = prev[E]; g.x[E] = x; g.a[E] = __builtin_fmaf(x * x, -0.1029432397f, -2.302208198f); }
    if constexpr (ST == 1) g.a[E] = __builtin_amdgcn_exp2f(g.x[E] * g.a[E]);
    if constexpr (ST == 2) g.a[E] = __builtin_amdgcn_rcpf(1.0f + g.a[E]);
    if constexpr (ST == 3) g.x[E] = g.x[E] * g.a[E];
  } else {
    if constexpr (ST == 0) { float x = prev[E]; g.x[E] = x; g.a[E] = __builtin_amdgcn_exp2f(x * 1.44269504089f); }
    if constexpr (ST == 1) { float x = g.x[E]; g.a[E] = (x > 0.f ? x + 1.0f : g.a[E]); }
    if constexpr (ST == 2) { float p = g.a[E] + 1e-6f; g.x[E] = pm ? p : g.x[E]; }
    if constexpr (ST == 3) g.x[E] = one ? 1.0f : g.x[E];
  }
  if constexpr (ST == 4 && (E & 1)) g.pk[E >> 1] = pk2(g.x[E - 1], g.x[E]);
}
template <int EPI, int T, int NUM, int HF, int E = 0> DI void epi_step(const f32x16& prev, GS& g, bool pm, bool one) {
  if constexpr (E < 16) {
    constexpr int st = T - (E * NUM) / 16;
    if constexpr (st >= 0 && st < 5 && ((st >= 3) == (HF == 0))) epi_op<EPI, E, st>(prev, g, pm, one);
    epi_step<EPI, T, NUM, HF, E + 1>(prev, g, pm, one);
  }
}
template <int B> DI void frag_write(GS& g, LAS char* p) { *(LAS u32x4*)p = u32x4{g.pk[B], g.pk[B + 1], g.pk[B + 2], g.pk[B + 3]}; }
constexpr int rst(int u) { constexpr int R[7] = {0, 5, 10, 15, 20, 24, 24}; return R[u]; }
template <int I, int IE, int SL, int PAR, int IMG> DI void do_reads(CS& s) {
  if constexpr (I < IE) {
    constexpr int uu = I / 4, q = I % 4, ab = (SL * 6 + uu + 2 * PAR) % 4;
    constexpr int o = (SL == 2 ? 0 : SL * SLOT_B) + uu * 4096;
    if constexpr (IMG == 0) s.A[ab][q] = trd(s.an[SL == 2], o + (q >> 1) * 2048 + (q & 1) * 512);
    else s.A[ab][q] = trd(s.ap[SL == 2], o + q * 1024);
    do_reads<I + 1, IE, SL, PAR, IMG>(s);
  }
}
struct PC { int boff; int w0, w1c; int y0, y1; bool cpm, cone; u64 yf; int yfo; bool l0; };
struct PCfg { int img, ori, pori, init, epi, cepi, hasprev, loadb, loado; };
template <int G, int PAR, int IMG, int ORI, int PORI, int INIT, int EPI, int CEPI, int HASPREV, int LOADB, int YW, int WPIN>
DI void do_gap(const PC& pc, CS& s, GS& g, f32x16& prev, f32x16& binit) {
  if constexpr (G < 36) {
    constexpr int ks = G / 2, hf = G % 2, sl = ks / 6, u = ks % 6;
    constexpr int km = (ks + 16) % 18, abm = (ks + 2 + 2 * PAR) % 4;
    constexpr bool wr_prev = (sl == 1 && CEPI != 0) || (sl == 0 && WPIN);
    if constexpr (u == 0 && hf == 0) { if constexpr (YW && G == 0) WAITV(0); if constexpr (wr_prev) WL(1); else WL(0); CB(); BAR(); CB(); SB(); if constexpr (YW && G == 0) { st_flag(s.rws, pc.yfo, pc.yf, pc.l0); SB(); } }
    if constexpr (hf == 0 && u >= 2) { constexpr int n = rst(u) - 4 * (u - 2) - 5 + (LOADB ? 4 : 0); WL(n > 15 ? 15 : n); SB(); }
    if constexpr (ks >= 2 || HASPREV) {
      f32x16 c;
      if constexpr (ks == 2 && hf == 0) { if constexpr (INIT) c = binit; else for (int i = 0; i < 16; i++) c[i] = 0.f; } else c = s.acc;
      const bf16x8 a = cat(s.A[abm][2 * hf], s.A[abm][2 * hf + 1]);
      f32x16 d;
      if constexpr ((ks < 2 ? PORI : ORI) == 0) d = MFMA32(a, s.B[2 * km + hf], c); else d = MFMA32(s.B[2 * km + hf], a, c);
      if constexpr (ks == 1 && hf == 1) prev = d; else s.acc = d;
    }
    if constexpr (hf == 0) do_reads<rst(u), rst(u + 1), sl, PAR, IMG>(s);
    if constexpr (INIT && G == 1) {
#pragma unroll
      for (int m = 0; m < 4; m++) set4(binit, m, *(LAS f32x4*)(s.biasp + pc.boff + m * 32));
    }
    if constexpr (LOADB && hf == 1) { s.B[2 * ks] = *(LAS bf16x8*)(s.hr + (2 * ks) * 1024); s.B[2 * ks + 1] = *(LAS bf16x8*)(s.hr + (2 * ks + 1) * 1024); }
    if constexpr (CEPI != 0 && ks < 4) epi_step<CEPI, ks + 16, 16, hf>(prev, g, pc.cpm, pc.cone);
    if constexpr ((EPI == 1 || EPI == 2) && ks >= 2) epi_step<EPI, ks - 2, LOADB ? 5 : 16, hf>(prev, g, s.pm, s.one);
    if constexpr (CEPI != 0 && G == 10) frag_write<4>(g, s.lb + pc.w1c);
    if constexpr ((EPI == 1 || EPI == 2) && !LOADB && G == 34) frag_write<0>(g, s.lb + pc.w0);
    if constexpr (LOADB && G == 22) { frag_write<0>(g, s.lb + pc.w0); frag_write<4>(g, s.lb + pc.w0 + 1024); }
    if constexpr (EPI == 3 && G == 7) __builtin_amdgcn_raw_buffer_store_b128(pack8(prev, 0), s.rws, s.vy, pc.y0, SC1);
    if constexpr (EPI == 3 && G == 9) __builtin_amdgcn_raw_buffer_store_b128(pack8(prev, 1), s.rws, s.vy, pc.y1, SC1);
    SGB(0x8, 1); SGB(0x100, 8); SGB(0x200, 2); SGB(0x2, 24); SGB(0x40, 1);
    SB();
    do_gap<G + 1, PAR, IMG, ORI, PORI, INIT, EPI, CEPI, HASPREV, LOADB, YW, WPIN>(pc, s, g, prev, binit);
  }
}
#define PASS(PAR, IMG, ORI, PORI, INIT, EPI, CEPI, HASPREV, LOADB, YW, WPIN) do { STAMP(npass + 2); npass++; do_gap<0, PAR, IMG, ORI, PORI, INIT, EPI, CEPI, HASPREV, LOADB, YW, WPIN>(pc, s, g, prev, binit); } while (0)

DI int yoff(int b, int r, int j) {
  const int q = b / 9, b9 = b - 9 * q; const int hi = b9 >= 5; const int jo = 2 * q + hi, be = b9 - 5 * hi;
  return WS_YP + (((r * 8 + jo) * 8 + j) * 5 + be) * 2048;
}

DI int y_need(int j, int rot, int pi) {
  const int b0 = 9 * (j >> 1) + 5 * (j & 1);
  const int la = (b0 + 2 * pi) >> 2, lb = (b0 + 2 * pi + (pi < 2 ? 1 : 0)) >> 2;
  const int pa = la - rot + (la < rot ? 9 : 0), pb = lb - rot + (lb < rot ? 9 : 0);
  return (pi == 2 && (j & 1)) ? 0 : (pa > pb ? pa : pb) + 1;
}
DI int need_mask(int j, int rot, int lo) { return (y_need(j, rot, 0) >= lo ? 1 : 0) | (y_need(j, rot, 1) >= lo ? 2 : 0) | (y_need(j, rot, 2) >= lo ? 4 : 0); }
constexpr int YPRE = 24, YLATE = 0;
DI void y_issue(u32x4 (&yv)[3][8], rsrc_t rws, int mask, int vy, int ybase) {
#pragma unroll
  for (int pi = 0; pi < 3; pi++) { const int vo = ((mask >> pi) & 1) && (pi < 2 || vy < 2048) ? vy : OOB;
#pragma unroll
    for (int i = 0; i < 8; i++) aload16(yv[pi][i], rws, vo, ybase + i * 10240 + pi * 4096); }
}
DI void y_gate(u32x4 (&yv)[8], u32x2 b0, u32x2 b1, float (&g)[8]) {
  unsigned klo = 0x00003f80u, khi = 0x3f800000u; asm("" : "+s"(klo), "+s"(khi));
#pragma unroll
  for (int e = 0; e < 8; e++) g[e] = 0.f;
#pragma unroll
  for (int i = 0; i < 8; i++) { PIN(yv[i]);
#pragma unroll
    for (int e = 0; e < 4; e++) { const unsigned w = yv[i][e]; const bf16x2 v = __builtin_bit_cast(bf16x2, w);
      g[2 * e] = __builtin_amdgcn_fdot2_f32_bf16(v, __builtin_bit_cast(bf16x2, klo), g[2 * e], false);
      g[2 * e + 1] = __builtin_amdgcn_fdot2_f32_bf16(v, __builtin_bit_cast(bf16x2, khi), g[2 * e + 1], false); } }
#pragma unroll
  for (int e = 0; e < 4; e++) {
    auto sw = __builtin_amdgcn_permlane32_swap(__builtin_bit_cast(unsigned, g[e]), __builtin_bit_cast(unsigned, g[4 + e]), false, false);
    g[e] = __builtin_bit_cast(float, (unsigned)sw[0]); g[4 + e] = __builtin_bit_cast(float, (unsigned)sw[1]);
  }
#pragma unroll
  for (int e = 0; e < 8; e++) { const unsigned bw = (e < 4 ? b0 : b1)[(e >> 1) & 1];
    const float y = g[e] + ((e & 1) ? bfhi(bw) : bflo(bw));
    g[e] = y * __builtin_amdgcn_rcpf(1.0f + __builtin_amdgcn_exp2f(-1.44269504089f * y)); }
}

struct SS { LAS char* a[5]; LAS char* b; s16x4 A[2][5][2]; bf16x8 B[2]; f32x4 acc[5]; };
template <int SG> DI void s_steps(SS& q, rsrc_t rws, const PC& pc, u32x4 (&yv)[3][8], int mid, int vy, int ybase) {
  if constexpr (SG <= 18) {
    if constexpr (SG < 18 && SG % 3 == 0) {
      if constexpr (SG == 6) asm volatile("s_waitcnt vmcnt(%0)" ::"i"(YPRE) : "memory");
      WL(0); CB(); BAR(); CB(); SB();
      if constexpr (SG == 6) { st_flag(rws, pc.yfo, pc.yf, pc.l0); SB(); }
      if constexpr (SG == 0) { y_issue(yv, rws, mid, vy, ybase); SB(); }
    }
    if constexpr (SG >= 1) {
#pragma unroll
      for (int n = 0; n < 5; n++) {
        f32x4 c; if constexpr (SG == 1) c = f32x4{0.f, 0.f, 0.f, 0.f}; else c = q.acc[n];
        q.acc[n] = MFMA16(cat(q.A[(SG - 1) & 1][n][0], q.A[(SG - 1) & 1][n][1]), q.B[(SG - 1) & 1], c);
      }
    }
    if constexpr (SG < 18) {
      constexpr int o = (SG % 9) * 8192 - (SG % 9 >= 6 ? 49152 : 0);
      q.B[SG & 1] = *(LAS bf16x8*)(q.b + SG * 1024);
#pragma unroll
      for (int n = 0; n < 5; n++) { q.A[SG & 1][n][0] = trd(q.a[n], o); q.A[SG & 1][n][1] = trd(q.a[n], o + 1024); }
      if constexpr (SG % 9 == 5) { for (int n = 0; n < 5; n++) q.a[n] += 49152; }
      if constexpr (SG % 9 == 8) { for (int n = 0; n < 5; n++) q.a[n] -= 49152; }
      SGB(0x8, 1); SGB(0x100, 3); SGB(0x8, 1); SGB(0x100, 2); SGB(0x8, 1); SGB(0x100, 2); SGB(0x8, 1); SGB(0x100, 2); SGB(0x8, 1); SGB(0x100, 2);
    }
    SB();
    s_steps<SG + 1>(q, rws, pc, yv, mid, vy, ybase);
  }
}

DI void consumer(const Params& p, LAS char* smem, int wid, int lane, int r, int j, int rot, u64 tag, unsigned stamp_) {
  int npass = 0; (void)npass; STAMPC(40);
  CS s; GS g; f32x16 prev, binit; PC pc;
  const int th = wid & 1, cw = wid >> 1;
  const int gq = lane >> 4, q = (lane >> 2) & 3, pp = lane & 3, h = lane >> 5, g2 = gq & 1;
  {
    const int c = 4 * cw + 2 * g2 + (pp >> 1);
    s.an[0] = smem + RING_OFF + h * 1024 + q * 128 + ((c ^ (((q >> 1) & 1) << 2)) * 16) + (pp & 1) * 8;
    s.ap[0] = smem + RING_OFF + (4 * h + q) * 128 + ((c ^ (((q >> 1) & 1) << 2)) * 16) + (pp & 1) * 8;
    s.an[1] = s.an[0] + 2 * SLOT_B; s.ap[1] = s.ap[0] + 2 * SLOT_B; PIN(s.an[1]); PIN(s.ap[1]);
    s.lb = smem + lane * 16; s.hr = s.lb + th * 36864; s.biasp = smem + BIAS_OFF + cw * 128 + h * 16;
    s.rws = mkrs(p.ws, WS_END);
    s.vy = (2 * th + g2) * 512 + h * 256 + (lane & 15) * 16;
    s.pm = true; s.one = false;
  }
#pragma unroll
  for (int k = 0; k < 36; k++) s.B[k] = *(LAS bf16x8*)(smem + th * 36864 + k * 1024 + ((lane ^ (2 * (k & 3) + h)) * 16));
  const int ytab = yoff(lane < 36 ? lane : 0, r, j);
#define YOFF(L) do { const int b_ = 4 * (L) + 2 * cw; pc.y0 = __builtin_amdgcn_readlane(ytab, b_); pc.y1 = __builtin_amdgcn_readlane(ytab, b_ + 1); } while (0)
  const int hb = th * 36864 + cw * 2048;
  const int kb = th * 36864 + 24576;
  const int qb = PQ_OFF + th * 6144;
  pc.cpm = true; pc.cone = false; pc.y0 = pc.y1 = 0; pc.yf = tag; pc.yfo = WS_FY + (r * 8 + j) * 8; pc.l0 = wid == 0 && lane == 0;
  pc.boff = wrap9(rot) * 256; pc.w0 = 0; pc.w1c = 0;                       PASS(0, 0, 0, 0, 1, 0, 0, 0, 0, 0, 0);
  pc.boff = wrap9(1 + rot) * 256; pc.w0 = hb;                              PASS(1, 0, 0, 0, 1, 1, 0, 1, 0, 0, 0);
  pc.boff = wrap9(2 + rot) * 256; pc.w0 = hb + 4096; pc.w1c = hb + 1024;   PASS(0, 0, 0, 0, 1, 1, 1, 1, 0, 0, 1);
  pc.boff = wrap9(3 + rot) * 256; pc.w1c = pc.w0 + 1024; pc.w0 = hb + 8192; PASS(1, 0, 0, 0, 1, 1, 1, 1, 0, 0, 1);
  pc.w1c = pc.w0 + 1024; pc.w0 = hb + 3 * 4096;                            PASS(0, 0, 1, 0, 0, 1, 1, 1, 0, 0, 1);
  s.pm = (cw == 0); pc.w1c = pc.w0 + 1024; pc.w0 = kb + cw * 2048;         PASS(1, 0, 1, 1, 0, 2, 1, 1, 0, 0, 1);
  pc.cpm = s.pm; pc.w1c = pc.w0 + 1024; pc.w0 = kb + 4096 + cw * 2048;     PASS(0, 0, 1, 1, 0, 2, 2, 1, 0, 0, 1);
  pc.w1c = pc.w0 + 1024; pc.w0 = cw == 0 ? qb + 4096 : DUMMY_OFF;
  s.pm = ((lane + 8) & 31) < 16; s.one = (lane & 31) == 16;                PASS(1, 0, 0, 1, 0, 2, 2, 1, 0, 0, 1);
  pc.cpm = s.pm; pc.cone = s.one; pc.w1c = cw == 0 ? pc.w0 + 1024 : DUMMY_OFF; pc.w0 = qb + cw * 2048; s.pm = true; s.one = false;
  pc.boff = wrap9(4 + rot) * 256;                                          PASS(0, 0, 0, 0, 1, 2, 2, 1, 0, 0, 1);
  pc.cpm = true; pc.cone = false; pc.w1c = pc.w0 + 1024; pc.w0 = hb + 4 * 4096;
  pc.boff = wrap9(5 + rot) * 256;                                          PASS(1, 0, 0, 0, 1, 1, 2, 1, 0, 0, 1);
  pc.w1c = pc.w0 + 1024; pc.w0 = hb + 5 * 4096; pc.boff = wrap9(6 + rot) * 256;  PASS(0, 0, 0, 0, 1, 1, 1, 1, 0, 0, 1);
  pc.w1c = pc.w0 + 1024; pc.w0 = hb + 6 * 4096; pc.boff = wrap9(7 + rot) * 256;  PASS(1, 0, 0, 0, 1, 1, 1, 1, 0, 0, 1);
  pc.w1c = pc.w0 + 1024; pc.w0 = hb + 7 * 4096; pc.boff = wrap9(8 + rot) * 256;  PASS(0, 0, 0, 0, 1, 1, 1, 1, 0, 0, 1);
  pc.w1c = pc.w0 + 1024; pc.w0 = hb + 8 * 4096;                            PASS(1, 1, 0, 0, 0, 1, 1, 1, 1, 0, 1);
  for (int i = 1; i < 9; i += 2) {
    YOFF(wrap9(i - 1 + rot));   PASS(0, 1, 0, 0, 0, 3, 0, 1, 0, 0, 0);
    YOFF(wrap9(i + rot)); pc.yf = tag + i;   PASS(1, 1, 0, 0, 0, 3, 0, 1, 0, 1, 0);
  }
  STAMP(24);
  {
    f32x16 d = MFMA32(cat(s.A[2][0], s.A[2][1]), s.B[32], s.acc); d = MFMA32(cat(s.A[2][2], s.A[2][3]), s.B[33], d);
    d = MFMA32(cat(s.A[3][0], s.A[3][1]), s.B[34], d); d = MFMA32(cat(s.A[3][2], s.A[3][3]), s.B[35], d);
    YOFF(wrap9(8 + rot));
    __builtin_amdgcn_raw_buffer_store_b128(pack8(d, 0), s.rws, s.vy, pc.y0, SC1);
    __builtin_amdgcn_raw_buffer_store_b128(pack8(d, 1), s.rws, s.vy, pc.y1, SC1);
    SB();
  }
  SS sq;
  {
    const int rho = 4 * g2 + q; const int wo0 = (288 * (j >> 1) + (j & 1) * 160) & 127;
#pragma unroll
    for (int n = 0; n < 5; n++) { const int o = wo0 + 32 * n; const int lc = o >> 7, tau = (o >> 5) & 3;
      sq.a[n] = smem + RING_OFF + lc * 4096 + (gq >> 1) * 2048 + rho * 128 + (((2 * tau + (pp >> 1)) ^ (((rho >> 1) & 3) << 1)) * 16) + (pp & 1) * 8; }
    sq.b = s.lb + wid * 18432;
  }
  const int c0 = 288 * (j >> 1) + (j & 1) * 160, tok = r * 64 + wid * 16 + (lane & 15);
  u32x2 xv[5], bv[5];
  {
    const rsrc_t rx = mkrs(p.x, 2048 * 1152), rb2 = mkrs(p.b2, 1152);
#pragma unroll
    for (int n = 0; n < 5; n++) { aload8p(xv[n], rx, tok * 1152 + gq * 8, c0 + 32 * n); aload8p(bv[n], rb2, gq * 8, c0 + 32 * n); }
  }
  pc.yf = tag + 9;
  const int own = need_mask(j, rot, 1), late = need_mask(j, rot, 8);
  const int vy = (gq >> 1) * 2048 + wid * 512 + g2 * 256 + (lane & 15) * 16, ybase = WS_YP + (r * 8 + j) * 81920;
  u32x4 yv[3][8];
  s_steps<0>(sq, s.rws, pc, yv, own & ~late, vy, ybase);
  STAMP(25);
  float gt[3][8];
#pragma unroll
  for (int e = 0; e < 8; e++) gt[2][e] = 0.f;
  WAITV(YLATE);
#pragma unroll
  for (int n = 0; n < 5; n++) PIN(bv[n]);
#pragma unroll
  for (int pi = 0; pi < 3; pi++) if (((own & ~late) >> pi) & 1) y_gate(yv[pi], bv[2 * pi], bv[pi < 2 ? 2 * pi + 1 : 4], gt[pi]);
  if (late) {
    poll_block(s.rws, (lane & 7) * 8, WS_FY + r * 64, tag, 9);
    STAMP(26);
    y_issue(yv, s.rws, late, vy, ybase);
    WAITV(0);
#pragma unroll
    for (int pi = 0; pi < 3; pi++) if ((late >> pi) & 1) y_gate(yv[pi], bv[2 * pi], bv[pi < 2 ? 2 * pi + 1 : 4], gt[pi]);
  }
  STAMP(27);
  const rsrc_t rout = mkrs(p.out, 2048 * 1152);
#pragma unroll
  for (int n = 0; n < 5; n++) {
    PIN(xv[n]);
    const float* gv = &gt[n >> 1][4 * (n & 1)];
    float o[4];
#pragma unroll
    for (int e = 0; e < 4; e++) { const unsigned xw = xv[n][e >> 1]; o[e] = __builtin_fmaf(sq.acc[n][e], gv[e], (e & 1) ? bfhi(xw) : bflo(xw)); }
    __builtin_amdgcn_raw_buffer_store_b64(u32x2{pk2(o[0], o[1]), pk2(o[2], o[3])}, rout, (n < 4 || (j & 1) == 0) ? tok * 1152 + gq * 8 : OOB, c0 + 32 * n, 0);
  }
  STAMP(28);
  STAMPC(41); if (wid == 0) STAMP_OUT(0);
}

constexpr int NA = NG1A;
constexpr int SQ0 = 3 * NA, SQ1 = SQ0 + 12;
constexpr int OS = 55;
struct LS { rsrc_t r1, r2, rw, ro; int vo1, vo2, voq, voq2, voo, voo2; unsigned d, sm; int j, r, rot, line0, k; };
DI int g1_so(const LS& c, int pi) { return c.j * 1152 + wrap9(pi + c.rot) * 128 + c.k * 64 * 9216; }
#define FIRE(E, N, AG, RS, LDS, VO, SO, STEP) do { const unsigned l_ = (LDS); const int s_ = (SO); asm volatile("" ::"s"(l_), "s"(s_)); \
    WAITV(8 + (E)); BAR(); CB(); dma_run<N, AG>(RS, l_, VO, s_, STEP); } while (0)
DI void ring_loader(const LS& c) {
  __builtin_amdgcn_s_setprio(RLPRIO);
  FIRE(0, 8, 0, c.r1, c.d + 2 * SLOT_B, c.vo1, g1_so(c, 0) + 2 * 192 * 9216, 8 * 9216);
  for (int pi = 1; pi < NA; pi++) { const int so = g1_so(c, pi);
#pragma unroll
    for (int t = 0; t < 3; t++) FIRE(0, 8, 0, c.r1, c.d + t * SLOT_B, c.vo1, so + t * 192 * 9216, 8 * 9216); }
  for (int q = 0; q < 4; q++) { const int so = WS_WQP + c.j * WQP_J + q * 3 * SLOT_B + c.k * 8192; const int vo = q == 2 ? c.voq2 : c.voq;
#pragma unroll
    for (int t = 0; t < 3; t++) FIRE(0, 8, 1, c.rw, c.d + t * SLOT_B, vo, so + t * SLOT_B, 1024); }
  for (int pi = NA; pi < 9; pi++) { const int so = g1_so(c, pi);
#pragma unroll
    for (int t = 0; t < 3; t++) FIRE(0, 8, 0, c.r1, c.d + t * SLOT_B, c.vo1, so + t * 192 * 9216, 8 * 9216); }
  int rb[3];
#pragma unroll
  for (int t = 0; t < 3; t++) rb[t] = (c.j * 576 + wrap9(wrap9(t * 3 + c.k) + c.rot) * 64) * 1152;
  for (int pi = 0; pi < 6; pi++) { const int co = wrap9(pi + c.rot) * 128;
#pragma unroll
    for (int t = 0; t < 3; t++) FIRE(0, 8, 0, c.r2, c.d + t * SLOT_B, c.vo2, rb[t] + co, 8 * 1152); }
  const unsigned od = c.sm + c.k * 24576; const int oo = WS_OF + c.r * 73728 + c.k * 24576;
#pragma unroll
  for (int n = 0; n < 9; n++) { const int co = wrap9(6 + n / 3 + c.rot) * 128;
    FIRE(n == 0 ? 0 : (n == 1 ? 3 : 6), 8, 0, c.r2, c.d + (n % 3) * SLOT_B, c.vo2, rb[n % 3] + co, 8 * 1152);
    if (n < 8) dma_run<3, 1>(c.rw, od + n * 3072, c.voq, oo + n * 3072, 1024); }
#pragma unroll
  for (int n = 0; n < 6; n++) { const int sg = n * 3 + c.k; const unsigned d = c.d + (n % 3) * SLOT_B;
    if (sg < 16) { const int so = (72 * (sg >> 1) + 32 * (sg & 1)) * 1152 + c.line0 * 128;
      FIRE(n == 0 ? 3 : 0, 4, 0, c.ro, d, c.voo, so, 8 * 1152); dma_run<4, 0>(c.ro, d + 4096, c.voo, so + 128, 8 * 1152);
    } else { const int so = 288 * (sg - 16) * 1152 + c.line0 * 128;
      FIRE(0, 2, 0, c.ro, d, c.voo2, so, 4 * 1152);
#pragma unroll
      for (int q = 1; q < 4; q++) dma_run<2, 0>(c.ro, d + q * 2048, c.voo2, so + (q >> 1) * 128 + (q & 1) * 144 * 1152, 4 * 1152);
    } }
  WAITV(8); BAR();
  WAITV(0); BAR();
}

DI void kv_pair(LAS char* lb, rsrc_t rws, int ta, int tb, int lane, int so) {
  const int e32 = lane & 31, h = lane >> 5;
  const int oa = ta == 4 ? PQ_OFF + 4096 : 24576 + ta * 2048, sa = ta == 4 ? 6144 : 36864;
  const int ob = tb == 4 ? PQ_OFF + 4096 : 24576 + tb * 2048, sb = tb == 4 ? 6144 : 36864;
  f32x16 d; for (int i = 0; i < 16; i++) d[i] = 0.f;
#pragma unroll
  for (int k = 0; k < 4; k++)
    d = MFMA32(*(LAS bf16x8*)(lb + oa + (k >> 1) * sa + (k & 1) * 1024), *(LAS bf16x8*)(lb + ob + (k >> 1) * sb + (k & 1) * 1024), d);
  const int e = tb == 4 ? (e32 == 16 ? 72 : (e32 >= 8 && e32 < 16 ? 56 + e32 : -1)) : (tb >> 1) * 32 + e32;
  const int hd = e / 18;
#pragma unroll
  for (int m = 0; m < 4; m++) { const int d0 = (ta >> 1) * 32 + 8 * m + 4 * h;
    const bool need = e >= 0 && d0 < 72 && (e == 72 || (d0 + 3 >= 18 * hd && d0 < 18 * hd + 18));
    __builtin_amdgcn_raw_buffer_store_b128(__builtin_bit_cast(u32x4, f32x4{d[4 * m], d[4 * m + 1], d[4 * m + 2], d[4 * m + 3]}), rws, need ? lane * 16 : OOB, so + m * 1024, SC1); }
}
DI bool vm_busy() { return (__builtin_amdgcn_s_getreg(7 | (31 << 11)) & 0x00C0000Fu) != 0; }
#define JOIN() do { CB(); BAR(); CB(); nb++; } while (0)
#define UNTIL(X) do { while (nb <= (X)) JOIN(); } while (0)
#define POLITE(LIM) do { while (vm_busy() && nb <= (LIM)) JOIN(); WAITV(0); } while (0)
#define SKIP(N, LIM) do { for (int k_ = 0; k_ < (N) && nb <= (LIM); k_++) JOIN(); } while (0)
#define POLL(VO, SO, NEED, FIRST, LIM) do { bool ok_ = false; SKIP(FIRST, LIM); \
    while (nb <= (LIM)) { u32x2 f_; aload8(f_, rws, VO, SO); JOIN(); POLITE(LIM); PIN(f_); if (tag_ok(f_, tag, NEED)) { ok_ = true; break; } SKIP(1, LIM); } \
    if (!ok_) poll_block(rws, VO, SO, tag, NEED); } while (0)

DI void o_half(LAS char* smem, rsrc_t rws, const u32x4 (&a)[12], int th, int lane, int r, int j) {
  const int h = lane >> 5;
  bf16x8 b[5];
#pragma unroll
  for (int i = 0; i < 4; i++) b[i] = *(LAS bf16x8*)(smem + PQ_OFF + th * 6144 + i * 1024 + lane * 16);
  b[4] = cat(trd(smem + PQ_OFF + th * 6144 + 4096 + ((lane >> 4) & 1) * 1024 + (24 + 4 * h + ((lane >> 2) & 3) + 32 * (lane & 1)) * 16 + ((lane >> 1) & 1) * 8, 0), s16x4{0, 0, 0, 0});
  f32x16 d2, de[2]; for (int i = 0; i < 16; i++) { d2[i] = 0.f; de[0][i] = 0.f; de[1][i] = 0.f; }
#pragma unroll
  for (int i = 0; i < 5; i++) d2 = MFMA32(__builtin_bit_cast(bf16x8, a[7 + i]), b[i], d2);
#pragma unroll
  for (int i = 0; i < 3; i++) de[0] = MFMA32(__builtin_bit_cast(bf16x8, a[i]), b[i], de[0]);
#pragma unroll
  for (int i = 0; i < 4; i++) de[1] = MFMA32(__builtin_bit_cast(bf16x8, a[3 + i]), b[i + 1], de[1]);
  float rd[4];
#pragma unroll
  for (int i = 0; i < 4; i++) rd[i] = __builtin_amdgcn_rcpf(d2[4 + i] + 1e-6f);
  const int ob = WS_OF + (r * 4 + 2 * th) * 18432;
  const int vo = ((lane >> 4) & 1) * 18432 + ((lane & 15) + 16 * h) * 16;
#pragma unroll
  for (int cc = 0; cc < 2; cc++) {
#pragma unroll
    for (int e = 0; e < 16; e++) { const int e0 = cc * 32 + (e & 3) + 8 * (e >> 2);
      const float r0 = rd[e0 / 18], r1 = rd[(e0 + 4) / 18]; de[cc][e] *= (e0 / 18 == (e0 + 4) / 18) ? r0 : (h ? r1 : r0); }
    __builtin_amdgcn_raw_buffer_store_b128(pack8(de[cc], 0), rws, vo, ob + (2 * j + cc) * 1024, SC1);
    __builtin_amdgcn_raw_buffer_store_b128(pack8(de[cc], 1), rws, vo, ob + (2 * j + cc) * 1024 + 512, SC1);
  }
  const int vo2 = ((lane >> 4) & 1) * 18432 + ((lane & 15) + 16 * (j & 3)) * 16 + 8 * h;
  __builtin_amdgcn_raw_buffer_store_b64(u32x2{pk2(d2[0] * rd[3], d2[1] * rd[3]), pk2(d2[2] * rd[3], d2[3] * rd[3])}, rws, vo2, ob + (16 + (j >> 2)) * 1024, SC1);
}

DI void service(const Params& p, LAS char* smem, rsrc_t rws, int lane, int r, int j, int rot, u64 tag, const u32x4 (&rp)[9], const int (&rpo)[9], unsigned stamp_) {
  const bool l0 = lane == 0;
  int nb = 0;
  UNTIL(0);
#pragma unroll
  for (int m = 0; m < 9; m++) __builtin_amdgcn_raw_buffer_store_b128(rp[m], rws, rpo[m], WS_WQP + j * WQP_J, SC1);
  JOIN(); POLITE(SQ0 - 3);
  st_flag(rws, WS_FREP + (j * 32 + r) * 8, tag, l0);
  UNTIL(SQ0 - 7); POLL((lane & 31) * 8, WS_FREP + j * 256, 0, 0, SQ0 - 3);
  STAMP(11);
  UNTIL(SQ1 + 2);
  __builtin_amdgcn_s_setprio(SVPRIO);
  STAMP(2);
  {
    const int kvo = WS_KVP + (j * 32 + r) * KVP_WG; LAS char* lb = smem + lane * 16;
    kv_pair(lb, rws, 0, 1, lane, kvo); kv_pair(lb, rws, 0, 3, lane, kvo + 4096); STAMP(3); JOIN();
    kv_pair(lb, rws, 2, 1, lane, kvo + 8192); kv_pair(lb, rws, 2, 3, lane, kvo + 12288); JOIN();
    kv_pair(lb, rws, 2, 4, lane, kvo + 16384); kv_pair(lb, rws, 0, 4, lane, kvo + 20480); JOIN();
    kv_pair(lb, rws, 4, 3, lane, kvo + 24576); kv_pair(lb, rws, 4, 4, lane, kvo + 28672);
  }
  constexpr int DL = OS - 1;
  SKIP(1, DL); POLITE(DL);
  st_flag(rws, WS_FKV1 + (j * 32 + r) * 8, tag, l0);
  STAMP(4);
  POLL((lane & 31) * 8, WS_FKV1 + j * 256, 0, 2, DL);
  STAMP(5);
  {
    const int P = r * 48 + (lane < 48 ? lane : 47);
    const int f = P >> 7, l2 = (P & 127) >> 1, half = P & 1, rho = l2 & 31, hh = l2 >> 5;
    const int T = f < 3 ? 0 : (f < 7 ? 1 : 2), ks = f < 3 ? f : (f < 7 ? f - 2 : f - 7);
    const int d0 = 16 * ks + 8 * half + 4 * hh;
    const int hd = T < 2 ? (32 * T + rho) / 18 : (rho < 8 ? 3 : (rho < 16 ? (rho & 3) : -9));
    const bool need = lane < 48 && d0 + 3 >= 18 * hd && d0 < 18 * hd + 18;
    const int ta = d0 >> 5;
    const int pair = T < 2 ? (ta == 2 ? 6 : 2 * ta + T) : (ta == 0 ? 5 : (ta == 1 ? 4 : 7));
    const int el = T < 2 ? rho : (rho < 8 ? 8 + rho : 16);
    const int vo = need ? pair * 4096 + ((d0 & 31) >> 3) * 1024 + (el + 32 * hh) * 16 : OOB;
    u32x4 v[32];
#pragma unroll
    for (int i = 0; i < 32; i++) aload16(v[i], rws, vo, WS_KVP + (j * 32 + i) * KVP_WG);
    SKIP(1, DL); POLITE(DL);
    float a[4];
#pragma unroll
    for (int i = 0; i < 32; i++) { PIN(v[i]);
#pragma unroll
      for (int e = 0; e < 4; e++) { const unsigned w = v[i][e]; const float fv = __builtin_bit_cast(float, w); a[e] = i == 0 ? fv : a[e] + fv; } }
#pragma unroll
    for (int e = 0; e < 4; e++) { const int d = d0 + e; a[e] = (d >= 18 * hd && d < 18 * hd + 18) ? a[e] : 0.f; }
    __builtin_amdgcn_raw_buffer_store_b64(u32x2{pk2(a[0], a[1]), pk2(a[2], a[3])}, rws, lane < 48 ? P * 8 : OOB, WS_KVF + j * 12288, SC1);
  }
  SKIP(1, DL); POLITE(DL);
  st_flag(rws, WS_FKV2 + (j * 32 + r) * 8, tag, l0);
  STAMP(6);
  POLL((lane & 31) * 8, WS_FKV2 + j * 256, 0, 2, DL);
  STAMP(7);
  {
    u32x4 a[12];
#pragma unroll
    for (int i = 0; i < 12; i++) aload16(a[i], rws, lane * 16, WS_KVF + j * 12288 + i * 1024);
    SKIP(1, DL); POLITE(DL);
    STAMP(8);
#pragma unroll
    for (int i = 0; i < 12; i++) PIN(a[i]);
    o_half(smem, rws, a, 0, lane, r, j); SKIP(1, DL);
    o_half(smem, rws, a, 1, lane, r, j);
  }
  STAMP(9);
  SKIP(1, DL); POLITE(DL);
  st_flag(rws, WS_FO + (r * 8 + j) * 8, tag, l0);
  POLL((lane & 7) * 8, WS_FO + r * 64, 0, 2, DL);
  STAMP(13);
  if (r == 0 && j == 0) st_flag(rws, WS_EPOCH, tag, l0);
  UNTIL(63);
  POLL((lane & 7) * 8, WS_FY + r * 64, 7, 0, 65);
  STAMP(14);
  UNTIL(71);
  STAMP_OUT(1);
}

__global__ __launch_bounds__(512, 2) NOPK void fused_main(Params p) {
  extern __shared__ __attribute__((aligned(16))) char smem_[];
  LAS char* smem = (LAS char*)smem_; const unsigned sm = (unsigned)(size_t)smem;
  const int tid = threadIdx.x, lane = tid & 63; const int wid = __builtin_amdgcn_readfirstlane(tid >> 6);
  const int j = (blockIdx.x & 6) | ((blockIdx.x >> 3) & 1), r = ((blockIdx.x >> 4) << 1) | (blockIdx.x & 1);
  const int rot = r % 9;
  unsigned stamp_ = 0; STAMP(0);
  {
    u64 pc; asm volatile("s_getpc_b64 %0" : "=s"(pc));
    const rsrc_t rc = mkrs((const void*)(pc & ~127ull), CODEPF * 1024);
    if (wid == 7) { const int ln = (blockIdx.x >> 3) * 16 + (lane & 15); unsigned t_;
      asm volatile("buffer_load_dword %0, %1, %2, 0 offen" : "=v"(t_) : "v"(lane < 16 ? ln * 128 : OOB), "s"(rc) : "memory");
      asm volatile("s_waitcnt vmcnt(0)" : "+v"(t_)::"memory"); }
  }
  const int tau = lane >> 3, li = lane & 7;
  const rsrc_t rx = mkrs(p.x, 2048 * 1152);
  u32x4 xv[9];
#pragma unroll
  for (int m = 0; m < 9; m++) xv[m] = __builtin_amdgcn_raw_buffer_load_b128(rx, (r * 64 + wid * 8 + tau) * 1152 + li * 16, m * 128, 0);
  LS c;
  c.rw = mkrs(p.ws, WS_END);
  u32x4 rp[9]; int rpo[9];
  if (wid == 7) {
    const rsrc_t rq = mkrs(p.wqkv, 576 * 3456);
#pragma unroll
    for (int m = 0; m < 9; m++) {
      const int idx = m * 64 + lane; const int row = 18 * r + (idx >> 5), ps = (idx >> 3) & 3, cp = idx & 7;
      const int cs = cp ^ (((row >> 1) & 1) << 2), cw = cs >> 2, c4 = cs & 3;
      const int col = ps < 2 ? (cw ? 1152 : 576) + 32 * ps + 8 * c4 : (ps == 3 ? 32 * cw + 8 * c4 : (c4 == 0 ? 576 : (c4 == 1 ? 1152 : 0)) + 64);
      const bool on = (ps != 2 || (cw == 0 && c4 != 2));
      rp[m] = __builtin_amdgcn_raw_buffer_load_b128(rq, on ? row * 3456 + (72 * j + col) * 2 : OOB, 0, 0);
      rpo[m] = on ? (ps * 18 + (row >> 5)) * 4096 + (row & 31) * 128 + cp * 16 : OOB;
    }
  }
  const u32x2 ep = __builtin_amdgcn_raw_buffer_load_b64(c.rw, 0, WS_EPOCH, SC1);
  const float bv0 = bf2f(p.b1[j * 576 + tid]); const float bv1 = bf2f(p.b1[j * 576 + 512 + (tid & 63)]);
  {
    const int c1 = li ^ (((tau >> 1) & 1) << 2), c2 = li ^ (((tau >> 1) & 3) << 1);
    c.r1 = mkrs(p.w1, 576 * 9216); c.r2 = mkrs(p.w2, 4608 * 1152); c.ro = mkrs(p.wo, 576 * 1152);
    c.vo1 = tau * 9216 + c1 * 16; c.vo2 = tau * 1152 + c1 * 16; c.voq = lane * 16; c.voq2 = (c1 < 4 && c1 != 2) ? c.voq : OOB;
    c.voo = tau * 1152 + c2 * 16;
    c.voo2 = (72 * (tau >> 2) + 64 + (tau & 3)) * 1152 + c2 * 16;
    c.k = wid - 4; c.sm = sm; c.d = sm + RING_OFF + c.k * 8192; c.j = j; c.r = r; c.rot = rot; c.line0 = (288 * (j >> 1) + (j & 1) * 160) >> 7;
    if (wid >= 4 && wid < 7) { dma_run<8, 0>(c.r1, c.d, c.vo1, g1_so(c, 0), 8 * 9216); dma_run<8, 0>(c.r1, c.d + SLOT_B, c.vo1, g1_so(c, 0) + 192 * 9216, 8 * 9216); }
  }
  {
    float sum = 0.f, sq = 0.f;
#pragma unroll
    for (int m = 0; m < 9; m++)
#pragma unroll
      for (int e = 0; e < 4; e++) { const unsigned w = xv[m][e]; const bf16x2 v = __builtin_bit_cast(bf16x2, w);
        sum = __builtin_amdgcn_fdot2_f32_bf16(v, __builtin_bit_cast(bf16x2, 0x3f803f80u), sum, false); sq = __builtin_amdgcn_fdot2_f32_bf16(v, v, sq, false); }
#define RED8(x) do { x += __builtin_bit_cast(float, __builtin_amdgcn_update_dpp(0, __builtin_bit_cast(int, x), 0xB1, 0xf, 0xf, true)); \
                     x += __builtin_bit_cast(float, __builtin_amdgcn_update_dpp(0, __builtin_bit_cast(int, x), 0x4E, 0xf, 0xf, true)); \
                     x += __builtin_bit_cast(float, __builtin_amdgcn_update_dpp(0, __builtin_bit_cast(int, x), 0x141, 0xf, 0xf, true)); } while (0)
    RED8(sum); RED8(sq);
    const float mean = sum * (1.0f / 576.0f);
    const float var = __builtin_fmaxf(__builtin_fmaf(-mean, mean, sq * (1.0f / 576.0f)), 0.f);
    const float rstd = __builtin_amdgcn_rsqf(var + 1e-5f), mb = -mean * rstd;
#pragma unroll
    for (int m = 0; m < 9; m++)
#pragma unroll
      for (int e = 0; e < 4; e++) { const unsigned w = xv[m][e]; xv[m][e] = pk2(__builtin_fmaf(bflo(w), rstd, mb), __builtin_fmaf(bfhi(w), rstd, mb)); }
#pragma unroll
    for (int m = 0; m < 9; m++) PIN(xv[m]);
    STAMP(30);
    WAITV(0); BAR(); CB(); STAMP(31);
    const int tk = wid * 8 + tau;
    LAS char* wb = smem + (tk >> 5) * 36864 + (li >> 1) * 1024 + ((((tk & 31) + 32 * (li & 1)) ^ li) * 16);
#pragma unroll
    for (int m = 0; m < 9; m++) *(LAS u32x4*)(wb + m * 4096) = xv[m];
    *(LAS float*)(smem + BIAS_OFF + tid * 4) = bv0; if (tid < 64) *(LAS float*)(smem + BIAS_OFF + 2048 + tid * 4) = bv1;
  }
  const u64 tag = (((u64)(unsigned)__builtin_amdgcn_readfirstlane(ep[1]) << 32) | (unsigned)__builtin_amdgcn_readfirstlane(ep[0])) + 16;
  WL(0); CB(); BAR(); CB();
  STAMP(1);
  if (wid < 4) consumer(p, smem, wid, lane, r, j, rot, tag, stamp_); else if (wid < 7) ring_loader(c); else service(p, smem, c.rw, lane, r, j, rot, tag, rp, rpo, stamp_);
}

extern "C" void kernel_launch(void* const* d_in, const int* in_sizes, int n_in, void* d_out, int out_size, void* d_ws, size_t ws_size, hipStream_t stream) {
  Params p;
  p.x = (const u16*)d_in[0]; p.wqkv = (const u16*)d_in[1]; p.wo = (const u16*)d_in[2]; p.w1 = (const u16*)d_in[4]; p.b1 = (const u16*)d_in[5];
  p.w2 = (const u16*)d_in[6]; p.b2 = (const u16*)d_in[7]; p.out = (u16*)d_out; p.ws = (char*)d_ws;
  (void)hipFuncSetAttribute((const void*)fused_main, hipFuncAttributeMaxDynamicSharedMemorySize, LDS_BYTES);
  fused_main<<<256, 512, LDS_BYTES, stream>>>(p);
}
```

```cpp
#include <hip/hip_runtime.h>
#include <hip/hip_bf16.h>

typedef unsigned short u16;
typedef unsigned long long u64;
typedef short s16x4 __attribute__((ext_vector_type(4)));
typedef __bf16 bf16x8 __attribute__((ext_vector_type(8)));
typedef __bf16 bf16x2 __attribute__((ext_vector_type(2)));
typedef float f32x2 __attribute__((ext_vector_type(2)));
typedef float f32x4 __attribute__((ext_vector_type(4)));
typedef float f32x16 __attribute__((ext_vector_type(16)));
typedef unsigned u32x2 __attribute__((ext_vector_type(2)));
typedef unsigned u32x4 __attribute__((ext_vector_type(4)));
typedef __amdgpu_buffer_rsrc_t rsrc_t;
#define LAS __attribute__((address_space(3)))
#if defined(__HIP_DEVICE_COMPILE__)
#define NOPK __attribute__((target("no-packed-fp32-ops")))
#else
#define NOPK
#endif
#define DI __device__ __forceinline__ NOPK
#define BAR() __builtin_amdgcn_s_barrier()
#define SB() __builtin_amdgcn_sched_barrier(0)
#define CB() asm volatile("" ::: "memory")
#define SGB(m, n) __builtin_amdgcn_sched_group_barrier(m, n, 0)
#define WAITV(n) asm volatile("s_waitcnt vmcnt(%0)" ::"i"(n) : "memory")
#define WL(n) __builtin_amdgcn_s_waitcnt(0xC07F | ((n) << 8))
#define MFMA32(a, b, c) __builtin_amdgcn_mfma_f32_32x32x16_bf16(a, b, c, 0, 0, 0)
#define MFMA16(a, b, c) __builtin_amdgcn_mfma_f32_16x16x32_bf16(a, b, c, 0, 0, 0)
#define SC1 16

constexpr unsigned WS_EPOCH = 0;
constexpr unsigned WS_FKV1 = 4096;
constexpr unsigned WS_FKV2 = WS_FKV1 + 4096;
constexpr unsigned WS_FREP = WS_FKV2 + 4096;
constexpr unsigned WS_FO = WS_FREP + 4096;
constexpr unsigned WS_FY = WS_FO + 4096;
constexpr unsigned WS_WQP = 32768;
constexpr unsigned WQP_J = 4 * 18 * 4096;
constexpr unsigned WS_KVP = WS_WQP + 8 * WQP_J;
constexpr unsigned KVP_WG = 6144;
constexpr unsigned WS_KVF = WS_KVP + 256 * KVP_WG;
constexpr unsigned WS_OF = WS_KVF + 8 * 12288;
constexpr unsigned WS_YP = WS_OF + 32 * 73728;
constexpr unsigned WS_END = WS_YP + 32 * 18 * 32768;
#define MAPMODE 1
#define SVPRIO 2
#define NG1A 4
#define YSV 3
#define YSG 9
#define ROT3 0
#define OSV 64
#define YLAST 12
#define YP2 68
#define PF1 3
#define SPIN 450
#define WPF 0
#define YPF 0
#define CODEPF 64
#define RLPRIO 0
constexpr int RING_OFF = 73728, SLOT_B = 24576, BIAS_OFF = 147456, PQ_OFF = 149760, DUMMY_OFF = 162048, TOUCH_OFF = 163072, LDS_BYTES = 163840;

DI float bf2f(u16 v) { return __builtin_bit_cast(float, ((unsigned)v) << 16); }
DI float bflo(unsigned v) { return __builtin_bit_cast(float, v << 16); }
DI float bfhi(unsigned v) { return __builtin_bit_cast(float, v & 0xffff0000u); }
DI unsigned pk2(float a, float b) { f32x2 v = {a, b}; bf16x2 r = __builtin_convertvector(v, bf16x2); return __builtin_bit_cast(unsigned, r); }
DI int wrap9(int v) { return v >= 9 ? v - 9 : v; }
DI int sig(int p, int r7) { const int v = p + r7; return v > 7 ? v - 7 : v; }
DI rsrc_t mkrs(const void* p, unsigned bytes) { return __builtin_amdgcn_make_buffer_rsrc((void*)p, 0, bytes, 0x00020000); }
#define DMA_NEXT(POL) "s_add_u32 m0, m0, 0x400\n\ts_add_u32 %0, %0, %4\n\tbuffer_load_dwordx4 %2, %3, %0 offen " POL "lds\n\t"
#define DMA_FIRST(POL) "s_mov_b32 m0, %1\n\ts_nop 0\n\tbuffer_load_dwordx4 %2, %3, %0 offen " POL "lds\n\t"
template <int N, int AG> DI void dma_run(rsrc_t rs, unsigned lds, int vo, int so, int step) {
  if constexpr (N == 8 && !AG) asm volatile(DMA_FIRST("") DMA_NEXT("") DMA_NEXT("") DMA_NEXT("") DMA_NEXT("") DMA_NEXT("") DMA_NEXT("") DMA_NEXT("") : "+s"(so) : "s"(lds), "v"(vo), "s"(rs), "s"(step) : "memory", "scc");
  if constexpr (N == 8 && AG) asm volatile(DMA_FIRST("sc1 ") DMA_NEXT("sc1 ") DMA_NEXT("sc1 ") DMA_NEXT("sc1 ") DMA_NEXT("sc1 ") DMA_NEXT("sc1 ") DMA_NEXT("sc1 ") DMA_NEXT("sc1 ") : "+s"(so) : "s"(lds), "v"(vo), "s"(rs), "s"(step) : "memory", "scc");
  if constexpr (N == 4) asm volatile(DMA_FIRST("") DMA_NEXT("") DMA_NEXT("") DMA_NEXT("") : "+s"(so) : "s"(lds), "v"(vo), "s"(rs), "s"(step) : "memory", "scc");
  if constexpr (N == 2 && AG) asm volatile(DMA_FIRST("sc1 ") DMA_NEXT("sc1 ") : "+s"(so) : "s"(lds), "v"(vo), "s"(rs), "s"(step) : "memory", "scc");
  if constexpr (N == 2 && !AG) asm volatile(DMA_FIRST("") DMA_NEXT("") : "+s"(so) : "s"(lds), "v"(vo), "s"(rs), "s"(step) : "memory", "scc");
}
DI void dma_two(rsrc_t rs, unsigned lds, int vo, int so) {
  asm volatile("s_mov_b32 m0, %1\n\ts_nop 0\n\tbuffer_load_dwordx4 %2, %3, %0 offen sc1 lds\n\ts_add_u32 m0, m0, 0x4800\n\ts_add_u32 %0, %0, 0x4800\n\tbuffer_load_dwordx4 %2, %3, %0 offen sc1 lds"
               : "+s"(so) : "s"(lds), "v"(vo), "s"(rs) : "memory", "scc");
}
DI s16x4 trd(LAS char* p, int off) { return __builtin_amdgcn_ds_read_tr16_b64_v4i16((LAS s16x4*)(p + off)); }
DI bf16x8 cat(s16x4 a, s16x4 b) { return __builtin_bit_cast(bf16x8, __builtin_shufflevector(a, b, 0, 1, 2, 3, 4, 5, 6, 7)); }
DI void set4(f32x16& v, int i, f32x4 x) { v[4 * i] = x[0]; v[4 * i + 1] = x[1]; v[4 * i + 2] = x[2]; v[4 * i + 3] = x[3]; }
DI u32x4 pack8(const f32x16& p, int t) {
  return u32x4{pk2(p[8 * t], p[8 * t + 1]), pk2(p[8 * t + 2], p[8 * t + 3]), pk2(p[8 * t + 4], p[8 * t + 5]), pk2(p[8 * t + 6], p[8 * t + 7])};
}
DI u32x4 packy(const f32x16& p, int t) {
  return u32x4{pk2(p[4 * t], p[4 * t + 1]), pk2(p[4 * t + 2], p[4 * t + 3]), pk2(p[8 + 4 * t], p[9 + 4 * t]), pk2(p[10 + 4 * t], p[11 + 4 * t])};
}
DI void aload16(u32x4& d, rsrc_t rs, int vo, int so) { asm volatile("buffer_load_dwordx4 %0, %1, %2, %3 offen sc1" : "=v"(d) : "v"(vo), "s"(rs), "s"(so) : "memory"); }
DI void aload16k(u32x4& d, rsrc_t rs, int vo, int so) { asm volatile("buffer_load_dwordx4 %0, %1, %2, %3 offen sc1" : "+v"(d) : "v"(vo), "s"(rs), "s"(so) : "memory"); }
DI void aload16p(u32x4& d, rsrc_t rs, int vo, int so) { asm volatile("buffer_load_dwordx4 %0, %1, %2, %3 offen" : "=v"(d) : "v"(vo), "s"(rs), "s"(so) : "memory"); }
DI void aload8(u32x2& d, rsrc_t rs, int vo, int so) { asm volatile("buffer_load_dwordx2 %0, %1, %2, %3 offen sc1" : "=v"(d) : "v"(vo), "s"(rs), "s"(so) : "memory"); }
DI void aload8p(u32x2& d, rsrc_t rs, int vo, int so) { asm volatile("buffer_load_dwordx2 %0, %1, %2, %3 offen" : "=v"(d) : "v"(vo), "s"(rs), "s"(so) : "memory"); }
constexpr int OOB = 0x7ffffff0;
#define PIN(x) asm volatile("" : "+v"(x))
DI bool tag_ok(u32x2 v, u64 tag, unsigned need) {
  const u64 d = (((u64)v[1] << 32) | v[0]) - tag;
  return __builtin_amdgcn_ballot_w64(d < need || d > 15) == 0;
}
DI void poll_block(rsrc_t rs, int vo, int so, u64 tag, unsigned need) {
#pragma unroll 1
  for (int it = 0; it < 20000; ++it) {
    u32x2 v; aload8(v, rs, vo, so); WAITV(0); PIN(v);
    if (tag_ok(v, tag, need)) break;
    __builtin_amdgcn_s_sleep(2);
  }
}
DI void st_flag(rsrc_t rs, int so, u64 v, bool on) { __builtin_amdgcn_raw_buffer_store_b64(u32x2{(unsigned)v, (unsigned)(v >> 32)}, rs, on ? 0 : OOB, so, SC1); }

#define STAMP(i)
#define STAMPC(i)
#define STAMP_OUT(w)
struct Params { const u16 *x, *wqkv, *wo, *w1, *b1, *w2, *b2; u16* out; char* ws; };

struct CS {
  bf16x8 B[36]; s16x4 A[4][4]; f32x16 acc;
  LAS char* an[2];
  LAS char* ap[2][2];
  LAS char* hl;
  LAS char* lb;
  LAS char* hr;
  LAS char* xr[4];
  LAS char* biasp;
  rsrc_t rws; int vy;
  bool pm, one;
};
struct GS { float x[16]; float a[16]; unsigned pk[8]; };
template <int EPI, int E, int ST> DI void epi_op(const f32x16& prev, GS& g, bool pm, bool one) {
  if constexpr (EPI == 1) {
    if constexpr (ST == 0) { float x = prev[E]; g.x[E] = x; g.a[E] = __builtin_fmaf(x * x, -0.1029432397f, -2.302208198f); }
    if constexpr (ST == 1) g.a[E] = __builtin_amdgcn_exp2f(g.x[E] * g.a[E]);
    if constexpr (ST == 2) g.a[E] = __builtin_amdgcn_rcpf(1.0f + g.a[E]);
    if constexpr (ST == 3) g.x[E] = g.x[E] * g.a[E];
  } else {
    if constexpr (ST == 0) { float x = prev[E]; g.x[E] = x; g.a[E] = __builtin_amdgcn_exp2f(x * 1.44269504089f); }
    if constexpr (ST == 1) { float x = g.x[E]; g.a[E] = (x > 0.f ? x + 1.0f : g.a[E]); }
    if constexpr (ST == 2) { float p = g.a[E] + 1e-6f; g.x[E] = pm ? p : g.x[E]; }
    if constexpr (ST == 3) g.x[E] = one ? 1.0f : g.x[E];
  }
  if constexpr (ST == 4 && (E & 1)) g.pk[E >> 1] = pk2(g.x[E - 1], g.x[E]);
}
template <int EPI, int T, int NUM, int HF, int E = 0> DI void epi_step(const f32x16& prev, GS& g, bool pm, bool one) {
  if constexpr (E < 16) {
    constexpr int st = T - (E * NUM) / 16;
    if constexpr (st >= 0 && st < 5 && (HF == 2 || (st >= 3) == (HF == 0))) epi_op<EPI, E, st>(prev, g, pm, one);
    epi_step<EPI, T, NUM, HF, E + 1>(prev, g, pm, one);
  }
}
template <int B> DI void frag_write(GS& g, LAS char* p) { *(LAS u32x4*)p = u32x4{g.pk[B], g.pk[B + 1], g.pk[B + 2], g.pk[B + 3]}; }
constexpr int rst(int u) { constexpr int R[7] = {0, 5, 10, 15, 20, 24, 24}; return R[u]; }
constexpr int rsg(int g) { constexpr int R[13] = {0, 3, 5, 8, 10, 13, 15, 18, 20, 22, 24, 24, 24}; return R[g]; }
template <int I, int IE, int SL, int PAR, int IMG> DI void do_reads(CS& s) {
  if constexpr (I < IE) {
    constexpr int uu = I / 4, q = I % 4, ab = (SL * 6 + uu + 2 * PAR) % 4;
    constexpr int o = (SL == 2 ? 0 : SL * SLOT_B) + uu * 4096;
    if constexpr (IMG == 0) s.A[ab][q] = trd(s.an[SL == 2], o + (q >> 1) * 2048 + (q & 1) * 512);
    else s.A[ab][q] = trd(s.ap[q >> 1][SL == 2], o + (q & 1) * 1024);
    do_reads<I + 1, IE, SL, PAR, IMG>(s);
  }
}
struct PC { int boff; int w0, w1c; int y0; bool cpm, cone; u64 yf; int yfo; bool l0; };
struct PCfg { int img, ori, pori, init, epi, cepi, hasprev, loadb, loado; };
DI f32x4 sub4(const f32x16& v, int i) { return f32x4{v[4 * i], v[4 * i + 1], v[4 * i + 2], v[4 * i + 3]}; }
template <int G, int PAR, int IMG, int ORI, int PORI, int INIT, int EPI, int CEPI, int HASPREV, int LOADB, int YW, int WPIN>
DI void do_gap(const PC& pc, CS& s, GS& g, f32x16& prev, f32x16& binit) {
  if constexpr (G < 36) {
    constexpr int ks = G / 2, hf = G % 2, sl = ks / 6, u = ks % 6;
    constexpr int km = (ks + 16) % 18, abm = (ks + 2 + 2 * PAR) % 4;
    constexpr bool m16 = IMG == 1 && (ks >= 2 || LOADB == 0);
    constexpr bool wr_prev = (sl == 1 && CEPI != 0) || (sl == 0 && WPIN);
    if constexpr (u == 0 && hf == 0) { if constexpr (YW && G == 0) WAITV(0); if constexpr (wr_prev) WL(1); else WL(0); CB(); BAR(); CB(); SB(); if constexpr (YW && G == 0) { st_flag(s.rws, pc.yfo, pc.yf, pc.l0); SB(); } }
    if constexpr (hf == 0 && u >= 2) { constexpr int n = rst(u) - 4 * (u - 2) - 5 + (LOADB ? 4 : 0); WL(n > 15 ? 15 : n); SB(); }
    if constexpr (ks >= 2 || HASPREV) {
      const bf16x8 a = cat(s.A[abm][2 * hf], s.A[abm][2 * hf + 1]);
      if constexpr (m16) {
        const f32x4 z = {0.f, 0.f, 0.f, 0.f};
        const f32x4 d0 = MFMA16(a, s.B[2 * km + hf], ks == 2 ? z : sub4(s.acc, 3 * hf)), d1 = MFMA16(a, s.B[2 * km + 1 - hf], ks == 2 ? z : sub4(s.acc, 1 + hf));
        if constexpr (ks == 1) { set4(prev, 3 * hf, d0); set4(prev, 1 + hf, d1); } else { set4(s.acc, 3 * hf, d0); set4(s.acc, 1 + hf, d1); }
      } else {
        f32x16 c;
        if constexpr (ks == 2 && hf == 0) { if constexpr (INIT) c = binit; else for (int i = 0; i < 16; i++) c[i] = 0.f; } else c = s.acc;
        f32x16 d;
        if constexpr ((ks < 2 ? PORI : ORI) == 0) d = MFMA32(a, s.B[2 * km + hf], c); else d = MFMA32(s.B[2 * km + hf], a, c);
        if constexpr (ks == 1 && hf == 1) prev = d; else s.acc = d;
      }
    }
    if constexpr (IMG == 1) do_reads<rsg(2 * u + hf), rsg(2 * u + hf + 1), sl, PAR, IMG>(s);
    else if constexpr (hf == 0) do_reads<rst(u), rst(u + 1), sl, PAR, IMG>(s);
    if constexpr (INIT && G == 1) {
#pragma unroll
      for (int m = 0; m < 4; m++) set4(binit, m, *(LAS f32x4*)(s.biasp + pc.boff + m * 32));
    }
    if constexpr (LOADB == 1 && hf == (u == 5 ? 0 : 1)) { s.B[2 * ks] = *(LAS bf16x8*)(s.hr + ks * 1024); s.B[2 * ks + 1] = *(LAS bf16x8*)(s.hr + (18 + ks) * 1024); }
    if constexpr (LOADB == 2 && hf == (u == 5 ? 0 : 1)) { s.B[2 * ks] = *(LAS bf16x8*)(s.xr[(2 * ks) & 3] + (2 * ks) * 1024); s.B[2 * ks + 1] = *(LAS bf16x8*)(s.xr[(2 * ks + 1) & 3] + (2 * ks + 1) * 1024); }
    if constexpr (CEPI != 0 && ks < 4) epi_step<CEPI, ks + 16, 16, hf>(prev, g, pc.cpm, pc.cone);
    if constexpr ((EPI == 1 || EPI == 2) && ks >= 2 && !(u == 5 && hf == 1)) epi_step<EPI, ks - 2, 16, u == 5 ? 2 : hf>(prev, g, s.pm, s.one);
    if constexpr (CEPI != 0 && G == 11) frag_write<4>(g, (CEPI == 1 ? s.hl + 512 : s.lb + 1024) + pc.w1c);
    if constexpr ((EPI == 1 || EPI == 2) && G == 35) frag_write<0>(g, (EPI == 1 ? s.hl : s.lb) + pc.w0);
    if constexpr (EPI == 3 && G == 7) __builtin_amdgcn_raw_buffer_store_b128(packy(prev, 0), s.rws, s.vy, pc.y0, SC1);
    if constexpr (EPI == 3 && G == 9) __builtin_amdgcn_raw_buffer_store_b128(packy(prev, 1), s.rws, s.vy, pc.y0 + 1024, SC1);
    if constexpr (m16) { SGB(0x8, 1); SGB(0x100, 2); SGB(0x8, 1); } else SGB(0x8, 1);
    SGB(0x100, 8); SGB(0x200, 2); SGB(0x2, 24); SGB(0x40, 1);
    SB();
    do_gap<G + 1, PAR, IMG, ORI, PORI, INIT, EPI, CEPI, HASPREV, LOADB, YW, WPIN>(pc, s, g, prev, binit);
  }
}
#define PASS(PAR, IMG, ORI, PORI, INIT, EPI, CEPI, HASPREV, LOADB, YW, WPIN) do { STAMP(npass + 2); npass++; do_gap<0, PAR, IMG, ORI, PORI, INIT, EPI, CEPI, HASPREV, LOADB, YW, WPIN>(pc, s, g, prev, binit); } while (0)

DI float silu(float y) { return y * __builtin_amdgcn_rcpf(1.0f + __builtin_amdgcn_exp2f(-1.44269504089f * y)); }
DI void y_add2(float* g, unsigned w, unsigned klo, unsigned khi) { const bf16x2 x = __builtin_bit_cast(bf16x2, w);
  g[0] = __builtin_amdgcn_fdot2_f32_bf16(x, __builtin_bit_cast(bf16x2, klo), g[0], false); g[1] = __builtin_amdgcn_fdot2_f32_bf16(x, __builtin_bit_cast(bf16x2, khi), g[1], false); }
DI void y_add(float* g, u32x4 v, unsigned klo, unsigned khi) {
#pragma unroll
  for (int e = 0; e < 4; e++) y_add2(g + 2 * e, v[e], klo, khi);
}

struct SS { LAS char* a[5]; LAS char* b; s16x4 A[2][5][2]; bf16x8 B[2]; f32x4 acc[5]; };
struct YT { u32x4 m[7][2], x[8]; float g[20]; int vy, vx, yb0, yb2, j, r7; unsigned klo, khi; LAS char* ownp; };
template <int SG> DI void s_steps(SS& q, rsrc_t rws, const PC& pc, YT& y) {
  if constexpr (SG <= 18) {
    if constexpr (SG < 18 && SG % 3 == 0) {
      if constexpr (SG == 0) WAITV(10);
      WL(0); CB(); BAR(); CB(); SB();
      if constexpr (SG == 0) { st_flag(rws, pc.yfo, pc.yf, pc.l0); SB(); }
    }
    if constexpr (SG == 1) {
#pragma unroll
      for (int p = 1; p < 5; p++) { const int so = y.yb0 + ((y.j - sig(p, y.r7)) & 7) * 4096; aload16(y.m[p - 1][0], rws, y.vy, so); aload16(y.m[p - 1][1], rws, y.vy, so + 32768); }
#pragma unroll
      for (int i = 0; i < 8; i++) aload16(y.x[i], rws, y.vx, y.yb2 + i * 4096);
      SB(); }
    if constexpr (SG == 4) {
      const u32x4 o0 = *(LAS u32x4*)y.ownp, o1 = *(LAS u32x4*)(y.ownp + 4096);
#pragma unroll
      for (int e = 0; e < 20; e++) y.g[e] = 0.f;
      y_add(y.g, o0, y.klo, y.khi); y_add(y.g + 8, o1, y.klo, y.khi); }
    if constexpr (SG == YLAST) {
#pragma unroll
      for (int p = 5; p < 8; p++) { const int so = y.yb0 + ((y.j - sig(p, y.r7)) & 7) * 4096; aload16(y.m[p - 1][0], rws, y.vy, so); aload16(y.m[p - 1][1], rws, y.vy, so + 32768); }
      SB(); }
    if constexpr (SG > YLAST && SG <= YLAST + 2) {
      constexpr int p = 2 * (SG - YLAST - 1);
      WAITV(18 - 2 * p); PIN(y.m[p][0]); PIN(y.m[p][1]); PIN(y.m[p + 1][0]); PIN(y.m[p + 1][1]); SB();
      y_add(y.g, y.m[p][0], y.klo, y.khi); y_add(y.g + 8, y.m[p][1], y.klo, y.khi); y_add(y.g, y.m[p + 1][0], y.klo, y.khi); y_add(y.g + 8, y.m[p + 1][1], y.klo, y.khi); }
    if constexpr (SG == YLAST + 3) {
      WAITV(6);
#pragma unroll
      for (int i = 0; i < 8; i++) PIN(y.x[i]);
      SB();
#pragma unroll
      for (int i = 0; i < 8; i++) { y_add2(y.g + 16, (y.j & 1) ? y.x[i][2] : y.x[i][0], y.klo, y.khi); y_add2(y.g + 18, (y.j & 1) ? y.x[i][3] : y.x[i][1], y.klo, y.khi); } }
    if constexpr (SG >= 1) {
#pragma unroll
      for (int n = 0; n < 5; n++) {
        f32x4 c; if constexpr (SG == 1) c = f32x4{0.f, 0.f, 0.f, 0.f}; else c = q.acc[n];
        q.acc[n] = MFMA16(cat(q.A[(SG - 1) & 1][n][0], q.A[(SG - 1) & 1][n][1]), q.B[(SG - 1) & 1], c);
      }
    }
    if constexpr (SG < 18) {
      constexpr int o = (SG % 9) * 8192 - (SG % 9 >= 6 ? 49152 : 0);
      q.B[SG & 1] = *(LAS bf16x8*)(q.b + SG * 1024);
#pragma unroll
      for (int n = 0; n < 5; n++) { q.A[SG & 1][n][0] = trd(q.a[n], o); q.A[SG & 1][n][1] = trd(q.a[n], o + 1024); }
      if constexpr (SG % 9 == 5) { for (int n = 0; n < 5; n++) q.a[n] += 49152; }
      if constexpr (SG % 9 == 8) { for (int n = 0; n < 5; n++) q.a[n] -= 49152; }
      SGB(0x8, 1); SGB(0x100, 3); SGB(0x8, 1); SGB(0x100, 2); SGB(0x8, 1); SGB(0x100, 2); SGB(0x8, 1); SGB(0x100, 2); SGB(0x8, 1); SGB(0x100, 2);
    }
    if constexpr (SG >= 4) { for (int e = 0; e < 20; e++) PIN(y.g[e]); }
    SB();
    s_steps<SG + 1>(q, rws, pc, y);
  }
}

DI void consumer(const Params& p, LAS char* smem, int wid, int lane, int r, int j, int rot, int r7, u64 tag, unsigned stamp_) {
  int npass = 0; (void)npass; STAMPC(40);
  CS s; GS g; f32x16 prev, binit; PC pc;
  const int th = wid & 1, cw = wid >> 1;
  const int gq = lane >> 4, q = (lane >> 2) & 3, pp = lane & 3, h = lane >> 5, g2 = gq & 1;
  {
    const int c = 4 * cw + 2 * g2 + (pp >> 1);
    s.an[0] = smem + RING_OFF + h * 1024 + q * 128 + ((c ^ (((q >> 1) & 1) << 2)) * 16) + (pp & 1) * 8;
    s.an[1] = s.an[0] + 2 * SLOT_B; PIN(s.an[1]);
    s.lb = smem + lane * 16; s.hr = s.lb + th * 36864; s.biasp = smem + BIAS_OFF + cw * 128 + h * 16;
    s.hl = smem + (2 * th + g2) * 18432 + cw * 1024 + ((lane & 15) + 16 * h) * 16;
    s.rws = mkrs(p.ws, WS_END);
    s.vy = th * 2048 + lane * 16;
    s.pm = true; s.one = false;
  }
#pragma unroll
  for (int k = 0; k < 4; k++) s.xr[k] = smem + th * 36864 + ((lane ^ (2 * k + h)) * 16);
#define YOFF(L) do { pc.y0 = WS_YP + ((r * 18 + 2 * (L) + cw) * 8 + j) * 4096; } while (0)
  constexpr int hb = 0;
  const int kb = th * 36864 + 14336;
  const int qb = PQ_OFF + th * 6144;
  pc.cpm = true; pc.cone = false; pc.y0 = 0; pc.yf = tag; pc.yfo = WS_FY + (r * 8 + j) * 8; pc.l0 = wid == 0 && lane == 0;
  pc.boff = wrap9(rot) * 256; pc.w0 = 0; pc.w1c = 0;                       PASS(0, 0, 0, 0, 1, 0, 0, 0, 2, 0, 0);
  pc.boff = wrap9(1 + rot) * 256; pc.w0 = hb;                              PASS(1, 0, 0, 0, 1, 1, 0, 1, 0, 0, 0);
#define G1P(i) do { pc.w1c = pc.w0; pc.w0 = hb + ((i) - 1) * 2048; pc.boff = wrap9((i) + rot) * 256; } while (0)
  G1P(2); PASS(0, 0, 0, 0, 1, 1, 1, 1, 0, 0, 1);
  G1P(3); PASS(1, 0, 0, 0, 1, 1, 1, 1, 0, 0, 1);
  constexpr int PA = NG1A & 1, PB = PA ^ 1;
  pc.w1c = pc.w0; pc.w0 = hb + (NG1A - 1) * 2048;                   PASS(PA, 0, 1, 0, 0, 1, 1, 1, 0, 0, 1);
  s.pm = (cw == 0); pc.w1c = pc.w0; pc.w0 = kb + cw * 2048;         PASS(PB, 0, 1, 1, 0, 2, 1, 1, 0, 0, 1);
  pc.cpm = s.pm; pc.w1c = pc.w0; pc.w0 = kb + 18432 + cw * 2048;     PASS(PA, 0, 1, 1, 0, 2, 2, 1, 0, 0, 1);
  pc.w1c = pc.w0; pc.w0 = cw == 0 ? qb + 4096 : DUMMY_OFF;
  s.pm = ((lane + 8) & 31) < 16; s.one = (lane & 31) == 16;
  pc.boff = wrap9(NG1A + rot) * 256;                                       PASS(PB, 0, 0, 1, 1, 2, 2, 1, 0, 0, 1);
  pc.cpm = s.pm; pc.cone = s.one; pc.w1c = cw == 0 ? pc.w0 : DUMMY_OFF - 1024; pc.w0 = hb + NG1A * 2048; s.pm = true; s.one = false;
  pc.boff = wrap9(NG1A + 1 + rot) * 256;                                   PASS(PA, 0, 0, 0, 1, 1, 2, 1, 0, 0, 1);
  pc.cpm = true; pc.cone = false;
  G1P(6); PASS(1, 0, 0, 0, 1, 1, 1, 1, 0, 0, 1);
  G1P(7); PASS(0, 0, 0, 0, 1, 1, 1, 1, 0, 0, 1);
  G1P(8); PASS(1, 0, 0, 0, 1, 1, 1, 1, 0, 0, 1);
  pc.w1c = pc.w0; pc.w0 = hb + 8 * 2048;                            PASS(0, 0, 0, 0, 0, 1, 1, 1, 0, 0, 1);
  {
    const int rho = 4 * g2 + q;
#pragma unroll
    for (int n = 0; n < 2; n++) { s.ap[n][0] = smem + RING_OFF + (gq >> 1) * 2048 + rho * 128 + (((2 * (2 * cw + n) + (pp >> 1)) ^ (((rho >> 1) & 3) << 1)) * 16) + (pp & 1) * 8;
      s.ap[n][1] = s.ap[n][0] + 2 * SLOT_B; PIN(s.ap[n][1]); }
  }
  pc.w1c = pc.w0; pc.w0 = qb + cw * 2048;                           PASS(1, 1, 0, 0, 0, 2, 1, 1, 1, 0, 1);
  pc.w1c = pc.w0; YOFF(8);                                                 PASS(0, 1, 0, 0, 0, 3, 2, 1, 0, 0, 1);
  YOFF((j + sig(1, r7)) & 7);                                              PASS(1, 1, 0, 0, 0, 3, 0, 1, 0, 0, 0);
  for (int i = 3; i < 7; i += 2) {
    YOFF((j + sig(i - 1, r7)) & 7);                                        PASS(0, 1, 0, 0, 0, 3, 0, 1, 0, 0, 0);
    YOFF((j + sig(i, r7)) & 7); pc.yf = tag + i;                           PASS(1, 1, 0, 0, 0, 3, 0, 1, 0, 1, 0);
  }
  YOFF((j + sig(6, r7)) & 7);                                              PASS(0, 1, 0, 0, 0, 3, 0, 1, 0, 0, 0);
  YOFF((j + sig(7, r7)) & 7);                                              PASS(1, 1, 0, 0, 0, 3, 0, 1, 0, 0, 0);
  const int tok = r * 64 + wid * 16 + (lane & 15);
  u32x2 xv[5], bv[5];
  {
    const rsrc_t rx = mkrs(p.x, 2048 * 1152), rb2 = mkrs(p.b2, 1152);
#pragma unroll
    for (int n = 0; n < 5; n++) { const int co = n < 4 ? 128 * j + 32 * n : 1024 + 32 * (j & 3);
      aload8p(xv[n], rx, tok * 1152 + gq * 8, co); aload8p(bv[n], rb2, gq * 8, co); }
  }
  STAMP(24);
  {
#pragma unroll
    for (int k = 0; k < 2; k++)
#pragma unroll
      for (int n = 0; n < 2; n++)
#pragma unroll
        for (int t = 0; t < 2; t++) set4(s.acc, 2 * n + t, MFMA16(cat(s.A[2 + k][2 * n], s.A[2 + k][2 * n + 1]), s.B[32 + 2 * k + t], sub4(s.acc, 2 * n + t)));
    *(LAS u32x4*)(smem + PQ_OFF + cw * 4096 + s.vy) = packy(s.acc, 0); *(LAS u32x4*)(smem + PQ_OFF + cw * 4096 + 1024 + s.vy) = packy(s.acc, 1);
    SB();
  }
  SS sq;
  {
    const int rho = 4 * g2 + q;
#pragma unroll
    for (int n = 0; n < 5; n++) { const int lc = n >> 2, tau = n < 4 ? n : (j & 3);
      sq.a[n] = smem + RING_OFF + lc * 4096 + (gq >> 1) * 2048 + rho * 128 + (((2 * tau + (pp >> 1)) ^ (((rho >> 1) & 3) << 1)) * 16) + (pp & 1) * 8; }
    sq.b = s.lb + wid * 18432;
  }
  pc.yf = tag + 8;
  YT y; y.vy = wid * 1024 + lane * 16; y.vx = j < 4 ? y.vy : OOB; y.j = j; y.r7 = r7; y.ownp = smem + PQ_OFF + y.vy;
  y.yb0 = WS_YP + (r * 18 + 2 * j) * 32768; y.yb2 = WS_YP + (r * 18 + 16 + ((j >> 1) & 1)) * 32768;
  y.klo = 0x00003f80u; y.khi = 0x3f800000u; asm("" : "+s"(y.klo), "+s"(y.khi));
  s_steps<0>(sq, s.rws, pc, y);
  STAMP(25);
  WAITV(0);
  STAMP(26);
#pragma unroll
  for (int p = 4; p < 7; p++) { PIN(y.m[p][0]); PIN(y.m[p][1]); y_add(y.g, y.m[p][0], y.klo, y.khi); y_add(y.g + 8, y.m[p][1], y.klo, y.khi); }
  STAMP(27);
  const rsrc_t rout = mkrs(p.out, 2048 * 1152);
#pragma unroll
  for (int n = 0; n < 5; n++) {
    PIN(xv[n]); PIN(bv[n]);
    float gv[4];
#pragma unroll
    for (int e = 0; e < 4; e++) { const unsigned bw = bv[n][e >> 1]; gv[e] = silu(y.g[4 * n + e] + ((e & 1) ? bfhi(bw) : bflo(bw))); }
    float o[4];
#pragma unroll
    for (int e = 0; e < 4; e++) { const unsigned xw = xv[n][e >> 1]; o[e] = __builtin_fmaf(sq.acc[n][e], gv[e], (e & 1) ? bfhi(xw) : bflo(xw)); }
    __builtin_amdgcn_raw_buffer_store_b64(u32x2{pk2(o[0], o[1]), pk2(o[2], o[3])}, rout, (n < 4 || j < 4) ? tok * 1152 + gq * 8 : OOB, n < 4 ? 128 * j + 32 * n : 1024 + 32 * (j & 3), SC1);
  }
  STAMP(28);
  STAMPC(41); if (wid == 0) STAMP_OUT(0);
}

constexpr int NA = NG1A;
constexpr int SQ0 = 3 * NA, SQ1 = SQ0 + 9;
constexpr int OS = OSV;
struct LS { rsrc_t r1, r2, rw, ro; int vo1, voq, voq2, voo, voo2; unsigned d, sm; int j, r, rot, r7, x8, k; };
DI int g1_so(const LS& c, int pi) { return c.j * 1152 + wrap9(pi + c.rot) * 128 + c.k * 64 * 9216; }
constexpr int ex_ops(int i) { return (i >= 64 && i < 70) ? 4 : 0; }
struct RingJobs { int rb[3]; };
template <int I> DI void ring_steps(const LS& c, RingJobs& q, LAS char* smem, int lane) {
  if constexpr (I < 70) {
    constexpr int n = I + 2, t = n % 3, E = 8 + ex_ops(I - 1);
    const unsigned d = c.d + t * SLOT_B;
    if constexpr (n < 66) {
      constexpr bool g1 = n < SQ0 || (n >= SQ1 && n < 36), qp = !g1 && n < 39;
      int so;
      if constexpr (g1) so = g1_so(c, (n < SQ0 ? n : n - 9) / 3) + t * 192 * 9216;
      else if constexpr (qp) so = WS_WQP + c.j * WQP_J + (n < SQ1 ? n - SQ0 : n - 27) * SLOT_B + c.k * 8192;
      else so = q.rb[t] + (n < 42 ? 8 : (n >= 63 ? c.j : ((c.j + sig((n - 39) / 3, c.r7)) & 7))) * 128;
      asm volatile("" ::"s"(d), "s"(so));
      WAITV(E); BAR(); CB();
      if constexpr (g1) dma_run<8, 0>(c.r1, d, c.vo1, so, 8 * 9216);
      else if constexpr (qp) dma_run<8, 1>(c.rw, d, (n >= SQ0 + 6 && n < SQ1) ? c.voq2 : c.voq, so, 1024);
      else dma_run<8, 0>(c.r2, d, c.voo, so, 8 * 1152);
    } else {
      const int sg = (n - 66) * 3 + c.k;
      const int so = (sg < 16 ? 72 * (sg >> 1) + 32 * (sg & 1) : 288 * (sg - 16)) * 1152;
      asm volatile("" ::"s"(d), "s"(so));
      WAITV(E); BAR(); CB();
      if (n < 71 || sg < 16) { dma_run<4, 0>(c.ro, d, c.voo, so + c.j * 128, 8 * 1152); dma_run<4, 0>(c.ro, d + 4096, c.voo, so + c.x8, 8 * 1152); }
      else {
#pragma unroll
        for (int u = 0; u < 4; u++) dma_run<2, 0>(c.ro, d + u * 2048, c.voo2, so + ((u >> 1) ? c.x8 : c.j * 128) + (u & 1) * 144 * 1152, 4 * 1152);
      }
    }
    if constexpr (I >= 64) {
      const int a = ((I - 64) * 3 + c.k) * 1024;
      dma_two(c.rw, c.sm + a, c.voq, WS_OF + c.r * 73728 + a); dma_two(c.rw, c.sm + a + 36864, c.voq, WS_OF + c.r * 73728 + a + 36864);
    }
    ring_steps<I + 1>(c, q, smem, lane);
  }
}
DI void ring_loader(const LS& c, LAS char* smem, int lane) {
  RingJobs q;
#pragma unroll
  for (int t = 0; t < 3; t++) q.rb[t] = (c.j * 576 + wrap9(t * 3 + c.k + c.rot) * 64) * 1152;
  ring_steps<0>(c, q, smem, lane);
  WAITV(8 + ex_ops(69)); BAR();
  WAITV(0); BAR();
}

template <int TA, int TB, int MM> DI void kv_pair(const bf16x8 (&f)[5][4], rsrc_t rws, int lane, int so) {
  const int e32 = lane & 31, h = lane >> 5;
  f32x16 d; for (int i = 0; i < 16; i++) d[i] = 0.f;
#pragma unroll
  for (int k = 0; k < 4; k++) d = MFMA32(f[TA][k], f[TB][k], d);
  const int e = TB == 4 ? (e32 == 16 ? 72 : (e32 >= 8 && e32 < 16 ? 56 + e32 : -99)) : (TB >> 1) * 32 + e32;
  const int q0 = (e / 18) * 18 / 4;
#pragma unroll
  for (int m = 0; m < 4; m++) if ((MM >> m) & 1) { const int dq = (TA >> 1) * 8 + 2 * m + h, qi = dq - q0;
    const int vo = e == 72 ? (360 + dq) * 16 : ((e >= 0 && qi >= 0 && qi < 5) ? (qi * 72 + e) * 16 : OOB);
    __builtin_amdgcn_raw_buffer_store_b128(__builtin_bit_cast(u32x4, f32x4{d[4 * m], d[4 * m + 1], d[4 * m + 2], d[4 * m + 3]}), rws, vo, so, SC1); }
}
DI void touch(rsrc_t rs, unsigned sm, int vo, int so) {
  asm volatile("s_mov_b32 m0, %0\n\ts_nop 0\n\tbuffer_load_dword %1, %2, %3 offen sc1 lds" ::"s"(sm + TOUCH_OFF), "v"(vo), "s"(rs), "s"(so) : "memory");
}

DI bool vm_busy() { return (__builtin_amdgcn_s_getreg(7 | (31 << 11)) & 0x00C0000Fu) != 0; }
#define JOIN() do { CB(); BAR(); CB(); nb++; } while (0)
#define UNTIL(X) do { while (nb <= (X)) JOIN(); } while (0)
#define SKIP(N, LIM) do { for (int k_ = 0; k_ < (N) && nb <= (LIM); k_++) JOIN(); } while (0)
#define POLITE(LIM) do { SKIP(1, LIM); while (vm_busy() && nb <= (LIM)) JOIN(); WAITV(0); } while (0)
#define POLL(VO, SO, NEED, FIRST, LIM) do { bool ok_ = false; SKIP(FIRST, LIM); \
    while (nb <= (LIM)) { u32x2 f_; aload8(f_, rws, VO, SO); POLITE(LIM); PIN(f_); if (tag_ok(f_, tag, NEED)) { ok_ = true; break; } SKIP(1, LIM); } \
    if (!ok_) poll_block(rws, VO, SO, tag, NEED); } while (0)

DI void o_mul(LAS char* smem, const u32x4 (&a)[12], int th, int lane, f32x16& d2, f32x16 (&de)[2]) {
  const int h = lane >> 5;
  bf16x8 b[5];
#pragma unroll
  for (int i = 0; i < 4; i++) b[i] = *(LAS bf16x8*)(smem + PQ_OFF + th * 6144 + i * 1024 + lane * 16);
  b[4] = cat(trd(smem + PQ_OFF + th * 6144 + 4096 + ((lane >> 4) & 1) * 1024 + (24 + 4 * h + ((lane >> 2) & 3) + 32 * (lane & 1)) * 16 + ((lane >> 1) & 1) * 8, 0), s16x4{0, 0, 0, 0});
  for (int i = 0; i < 16; i++) { d2[i] = 0.f; de[0][i] = 0.f; de[1][i] = 0.f; }
#pragma unroll
  for (int i = 0; i < 5; i++) d2 = MFMA32(__builtin_bit_cast(bf16x8, a[7 + i]), b[i], d2);
#pragma unroll
  for (int i = 0; i < 3; i++) de[0] = MFMA32(__builtin_bit_cast(bf16x8, a[i]), b[i], de[0]);
#pragma unroll
  for (int i = 0; i < 4; i++) de[1] = MFMA32(__builtin_bit_cast(bf16x8, a[3 + i]), b[i + 1], de[1]);
  PIN(d2); PIN(de[0]); PIN(de[1]);
}
DI void o_out(rsrc_t rws, int th, int lane, int r, int j, const f32x16& d2, f32x16 (&de)[2]) {
  const int h = lane >> 5;
  float rd[4];
#pragma unroll
  for (int i = 0; i < 4; i++) rd[i] = __builtin_amdgcn_rcpf(d2[4 + i] + 1e-6f);
  const int ob = WS_OF + (r * 4 + 2 * th) * 18432;
  const int vo = ((lane >> 4) & 1) * 18432 + ((lane & 15) + 16 * h) * 16;
#pragma unroll
  for (int cc = 0; cc < 2; cc++) {
#pragma unroll
    for (int e = 0; e < 16; e++) { const int e0 = cc * 32 + (e & 3) + 8 * (e >> 2);
      const float r0 = rd[e0 / 18], r1 = rd[(e0 + 4) / 18]; de[cc][e] *= (e0 / 18 == (e0 + 4) / 18) ? r0 : (h ? r1 : r0); }
    __builtin_amdgcn_raw_buffer_store_b128(pack8(de[cc], 0), rws, vo, ob + (2 * j + cc) * 1024, SC1);
    __builtin_amdgcn_raw_buffer_store_b128(pack8(de[cc], 1), rws, vo, ob + (2 * j + cc) * 1024 + 512, SC1);
  }
  const int vo2 = ((lane >> 4) & 1) * 18432 + ((lane & 15) + 16 * (j & 3)) * 16 + 8 * h;
  __builtin_amdgcn_raw_buffer_store_b64(u32x2{pk2(d2[0] * rd[3], d2[1] * rd[3]), pk2(d2[2] * rd[3], d2[3] * rd[3])}, rws, vo2, ob + (16 + (j >> 2)) * 1024, SC1);
}

DI void service(const Params& p, LAS char* smem, rsrc_t rws, int lane, int r, int j, u64 tag, u32x4 (&rp)[9], const int (&rpo)[2], unsigned stamp_) {
  const bool l0 = lane == 0;
  int nb = -1;
  WL(0);
  while (vm_busy() && nb <= SQ0 - 3) JOIN();
  WAITV(0);
#pragma unroll
  for (int m = 0; m < 9; m++) { PIN(rp[m]); const int row = 18 * r + 2 * m;
      __builtin_amdgcn_raw_buffer_store_b128(rp[m], rws, rpo[m & 1], WS_WQP + j * WQP_J + (row >> 5) * 4096 + (row & 31) * 128, SC1); }
  __builtin_amdgcn_raw_buffer_store_b128(u32x4{0u, 0u, 0u, 0u}, rws, lane < 24 ? lane * 16 : OOB, WS_KVF + j * 12288 + r * 384, SC1);
  POLITE(SQ0 - 3);
  st_flag(rws, WS_FREP + (j * 32 + r) * 8, tag, l0);
  UNTIL(SQ0 - 6);
  POLL((lane & 31) * 8, WS_FREP + j * 256, 0, 0, SQ0 - 3);
  {
    constexpr int NW = 32 >> MAPMODE, SH = 1152 / NW; const rsrc_t ro = mkrs(p.wo, 576 * 1152);
    UNTIL(8);
#pragma unroll
    for (int i = 0; i < SH; i += 64) { const int n = (r >> MAPMODE) * SH + i + lane;
      touch(mkrs(p.w2, 4608 * 1152), (unsigned)(size_t)smem, i + lane < SH ? (n >> 1) * 1152 + ((n & 1) ? 1024 : j * 128) : OOB, j * 663552); }
    SKIP(3, 20);
#pragma unroll
    for (int i = 0; i < SH; i += 64) { const int n = (r >> MAPMODE) * SH + i + lane;
      touch(ro, (unsigned)(size_t)smem, i + lane < SH ? (n >> 1) * 1152 + ((n & 1) ? 1024 : j * 128) : OOB, 0); }
  }
  STAMP(11);
  UNTIL(SQ1 + 5);
  __builtin_amdgcn_s_setprio(SVPRIO);
  STAMP(2);
  {
    const int kvo = WS_KVP + (j * 32 + r) * KVP_WG; LAS char* lb = smem + lane * 16;
    bf16x8 f[5][4];
#pragma unroll
    for (int t = 0; t < 5; t++)
#pragma unroll
      for (int k = 0; k < 4; k++) f[t][k] = *(LAS bf16x8*)(lb + (t == 4 ? PQ_OFF + 4096 + (k >> 1) * 6144 : 14336 + (t >> 1) * 18432 + (t & 1) * 2048 + (k >> 1) * 36864) + (k & 1) * 1024);
#pragma unroll
    for (int t = 0; t < 5; t++) for (int k = 0; k < 4; k++) PIN(f[t][k]);
    kv_pair<2, 1, 1>(f, rws, lane, kvo); kv_pair<4, 3, 1>(f, rws, lane, kvo); SB(); STAMP(3); JOIN();
    kv_pair<0, 1, 15>(f, rws, lane, kvo); kv_pair<0, 3, 12>(f, rws, lane, kvo); SB(); JOIN();
    kv_pair<2, 3, 15>(f, rws, lane, kvo); kv_pair<4, 4, 1>(f, rws, lane, kvo); SB(); JOIN();
    kv_pair<2, 4, 15>(f, rws, lane, kvo); kv_pair<0, 4, 15>(f, rws, lane, kvo);
  }
  constexpr int DL = OS - 1;
  POLITE(DL);
  st_flag(rws, WS_FKV1 + (j * 32 + r) * 8, tag, l0);
  STAMP(4);
  POLL((lane & 31) * 8, WS_FKV1 + j * 256, 0, PF1, DL);
  STAMP(5);
  {
    const int c = r * 12 + (lane & 15); const bool on = (lane & 15) < 12 && c < 378;
    u32x4 v[8];
#pragma unroll
    for (int i = 0; i < 8; i++) aload16(v[i], rws, on ? (lane >> 4) * 8 * KVP_WG + c * 16 : OOB, WS_KVP + (j * 32 + i) * KVP_WG);
    POLITE(DL);
    float a[4];
#pragma unroll
    for (int i = 0; i < 8; i++) { PIN(v[i]);
#pragma unroll
      for (int e = 0; e < 4; e++) { const unsigned w = v[i][e]; const float fv = __builtin_bit_cast(float, w); a[e] = i == 0 ? fv : a[e] + fv; } }
    const bool isz = c >= 360; const int e = c % 72, g4 = lane >> 4;
    const int dq = isz ? c - 360 : c / 72 + (e / 18) * 18 / 4;
    const int hd = isz ? (4 * dq + ((g4 & 2) ? 3 : 0)) / 18 : e / 18;
    const bool wr = on && (isz ? (g4 < 2 || (4 * dq) / 18 != hd) : g4 == 0);
    const int T = isz ? 2 : e >> 5, rho = isz ? 8 + 4 * (g4 & 1) + hd : e & 31;
#pragma unroll
    for (int k = 0; k < 4; k++) {
      auto s1 = __builtin_amdgcn_permlane16_swap(__builtin_bit_cast(unsigned, a[k]), __builtin_bit_cast(unsigned, a[k]), false, false);
      a[k] = __builtin_bit_cast(float, (unsigned)s1[0]) + __builtin_bit_cast(float, (unsigned)s1[1]);
      auto s2 = __builtin_amdgcn_permlane32_swap(__builtin_bit_cast(unsigned, a[k]), __builtin_bit_cast(unsigned, a[k]), false, false);
      a[k] = __builtin_bit_cast(float, (unsigned)s2[0]) + __builtin_bit_cast(float, (unsigned)s2[1]);
      const int d = 4 * dq + k; a[k] = (d >= 18 * hd && d < 18 * hd + 18) ? a[k] : 0.f;
    }
    const int f = (dq >> 2) + (T == 0 ? 0 : (T == 1 ? 2 : 7));
    __builtin_amdgcn_raw_buffer_store_b64(u32x2{pk2(a[0], a[1]), pk2(a[2], a[3])}, rws, wr ? f * 1024 + (rho + 32 * (dq & 1)) * 16 + ((dq >> 1) & 1) * 8 : OOB, WS_KVF + j * 12288, SC1);
  }
  POLITE(DL);
  st_flag(rws, WS_FKV2 + (j * 32 + r) * 8, tag, l0);
  STAMP(6);
  POLL((lane & 31) * 8, WS_FKV2 + j * 256, 0, PF1, DL);
  STAMP(7);
  {
    u32x4 a[12];
#pragma unroll
    for (int i = 0; i < 12; i++) { aload16(a[i], rws, lane * 16, WS_KVF + j * 12288 + i * 1024); if (i == 5) SKIP(1, DL); }
    POLITE(DL); UNTIL(44);
    STAMP(8);
#pragma unroll
    for (int i = 0; i < 12; i++) PIN(a[i]);
    f32x16 d2, de[2];
    o_mul(smem, a, 0, lane, d2, de); SKIP(1, DL);
    o_out(rws, 0, lane, r, j, d2, de); SB(); SKIP(1, DL);
    o_mul(smem, a, 1, lane, d2, de); SKIP(1, DL);
    o_out(rws, 1, lane, r, j, d2, de);
  }
  STAMP(9);
  POLITE(DL);
  st_flag(rws, WS_FO + (r * 8 + j) * 8, tag, l0);
  POLL((lane & 7) * 8, WS_FO + r * 64, 0, PF1, DL);
  STAMP(13);
  if (r == 0 && j == 0) st_flag(rws, WS_EPOCH, tag, l0);
  UNTIL(60);
  POLL((lane & 7) * 8, WS_FY + r * 64, 5, 0, 65);
  STAMP(14);
  UNTIL(YP2);
  POLL((lane & 7) * 8, WS_FY + r * 64, 8, 0, 65 + YLAST / 3);
  STAMP(17);
  UNTIL(71);
  STAMP_OUT(1);
}

__global__ __launch_bounds__(512, 2) NOPK void fused_main(Params p) {
  extern __shared__ __attribute__((aligned(16))) char smem_[];
  LAS char* smem = (LAS char*)smem_; const unsigned sm = (unsigned)(size_t)smem;
  const int tid = threadIdx.x, lane = tid & 63; const int wid = __builtin_amdgcn_readfirstlane(tid >> 6);
  const int j = (blockIdx.x & 6) | ((blockIdx.x >> 3) & 1), r = ((blockIdx.x >> 4) << 1) | (blockIdx.x & 1);
  const int rot = (r >> MAPMODE) % 9, r7 = (r >> MAPMODE) % 7;
  unsigned stamp_ = 0; STAMP(0);
  if (wid == 6) {
    u64 pc; asm volatile("s_getpc_b64 %0" : "=s"(pc));
    touch(mkrs((const void*)(pc & ~127ull), CODEPF * 1024), sm, lane < 16 ? ((blockIdx.x >> 3) * 16 + lane) * 128 : OOB, 0);
  }
  const int tau = lane >> 3, li = lane & 7;
  const rsrc_t rws = mkrs(p.ws, WS_END);
  if (wid < 4) {
    const rsrc_t rx = mkrs(p.x, 2048 * 1152);
    u32x4 xv[2][9];
#pragma unroll
    for (int a = 0; a < 2; a++)
#pragma unroll
      for (int m = 0; m < 9; m++) xv[a][m] = __builtin_amdgcn_raw_buffer_load_b128(rx, (r * 64 + wid * 16 + a * 8 + tau) * 1152 + li * 16, m * 128, 0);
    const u32x2 ep = __builtin_amdgcn_raw_buffer_load_b64(rws, 0, WS_EPOCH, SC1);
#pragma unroll
    for (int a = 0; a < 2; a++) {
      float sum = 0.f, sq = 0.f;
#pragma unroll
      for (int m = 0; m < 9; m++)
#pragma unroll
        for (int e = 0; e < 4; e++) { const unsigned w = xv[a][m][e]; const bf16x2 v = __builtin_bit_cast(bf16x2, w);
          sum = __builtin_amdgcn_fdot2_f32_bf16(v, __builtin_bit_cast(bf16x2, 0x3f803f80u), sum, false); sq = __builtin_amdgcn_fdot2_f32_bf16(v, v, sq, false); }
#define RED8(x) do { x += __builtin_bit_cast(float, __builtin_amdgcn_update_dpp(0, __builtin_bit_cast(int, x), 0xB1, 0xf, 0xf, true)); \
                     x += __builtin_bit_cast(float, __builtin_amdgcn_update_dpp(0, __builtin_bit_cast(int, x), 0x4E, 0xf, 0xf, true)); \
                     x += __builtin_bit_cast(float, __builtin_amdgcn_update_dpp(0, __builtin_bit_cast(int, x), 0x141, 0xf, 0xf, true)); } while (0)
      RED8(sum); RED8(sq);
      const float mean = sum * (1.0f / 576.0f);
      const float var = __builtin_fmaxf(__builtin_fmaf(-mean, mean, sq * (1.0f / 576.0f)), 0.f);
      const float rstd = __builtin_amdgcn_rsqf(var + 1e-5f), mb = -mean * rstd;
      const int tk = wid * 16 + a * 8 + tau;
      LAS char* wb = smem + (tk >> 5) * 36864 + (li >> 1) * 1024 + ((((tk & 31) + 32 * (li & 1)) ^ li) * 16);
#pragma unroll
      for (int m = 0; m < 9; m++) {
        u32x4 o;
#pragma unroll
        for (int e = 0; e < 4; e++) { const unsigned w = xv[a][m][e]; o[e] = pk2(__builtin_fmaf(bflo(w), rstd, mb), __builtin_fmaf(bfhi(w), rstd, mb)); }
        *(LAS u32x4*)(wb + m * 4096) = o;
      }
      SB();
    }
    const u64 tag = (((u64)(unsigned)__builtin_amdgcn_readfirstlane(ep[1]) << 32) | (unsigned)__builtin_amdgcn_readfirstlane(ep[0])) + 16;
    STAMP(30); STAMP(31);
    WL(0); CB(); BAR(); CB();
    STAMP(1);
    consumer(p, smem, wid, lane, r, j, rot, r7, tag, stamp_);
  } else if (wid < 7) {
    LS c;
    const int c1 = li ^ (((tau >> 1) & 1) << 2), c2 = li ^ (((tau >> 1) & 3) << 1);
    c.r1 = mkrs(p.w1, 576 * 9216); c.vo1 = tau * 9216 + c1 * 16;
    c.k = wid - 4; c.sm = sm; c.d = sm + RING_OFF + c.k * 8192; c.j = j; c.r = r; c.rot = rot; c.r7 = r7;
    dma_run<8, 0>(c.r1, c.d, c.vo1, g1_so(c, 0), 8 * 9216); dma_run<8, 0>(c.r1, c.d + SLOT_B, c.vo1, g1_so(c, 0) + 192 * 9216, 8 * 9216);
    c.rw = rws; c.r2 = mkrs(p.w2, 4608 * 1152); c.ro = mkrs(p.wo, 576 * 1152);
    c.voq = lane * 16; c.voq2 = (c1 < 4 && c1 != 2) ? c.voq : OOB;
    c.voo = tau * 1152 + c2 * 16; c.voo2 = (72 * (tau >> 2) + 64 + (tau & 3)) * 1152 + c2 * 16;
    c.x8 = j < 4 ? 1024 : 0x40000000;
    BAR();
    ring_loader(c, smem, lane);
  } else {
    u32x4 b1a, b1b; u32x2 ep;
    { const rsrc_t rb1 = mkrs(p.b1, 9216); aload16p(b1a, rb1, lane * 16, j * 1152); aload16p(b1b, rb1, lane < 8 ? lane * 16 : OOB, j * 1152 + 1024); aload8(ep, rws, 0, WS_EPOCH); }
    u32x4 rp[9]; int rpo[2];
    {
      const rsrc_t rq = mkrs(p.wqkv, 576 * 3456); const int ps = (lane >> 3) & 3; int src[2];
#pragma unroll
      for (int par = 0; par < 2; par++) {
        const int cs = li ^ (par << 2), cw = cs >> 2, c4 = cs & 3;
        const int col = ps < 2 ? (cw ? 1152 : 576) + 32 * ps + 8 * c4 : (ps == 3 ? 32 * cw + 8 * c4 : (c4 == 0 ? 576 : (c4 == 1 ? 1152 : 0)) + 64);
        const bool on = (ps != 2 || (cw == 0 && c4 != 2));
        src[par] = on ? (lane >> 5) * 3456 + col * 2 : OOB; rpo[par] = on ? ps * 73728 + (lane >> 5) * 128 + li * 16 : OOB;
      }
      if (r & 1) { const int t = src[0]; src[0] = src[1]; src[1] = t; const int u = rpo[0]; rpo[0] = rpo[1]; rpo[1] = u; }
#pragma unroll
      for (int m = 0; m < 9; m++) aload16p(rp[m], rq, src[m & 1], (18 * r + 2 * m) * 3456 + 144 * j);
    }
    WAITV(9); PIN(b1a); PIN(b1b); PIN(ep);
    *(LAS f32x4*)(smem + BIAS_OFF + lane * 32) = f32x4{bflo(b1a[0]), bfhi(b1a[0]), bflo(b1a[1]), bfhi(b1a[1])};
    *(LAS f32x4*)(smem + BIAS_OFF + lane * 32 + 16) = f32x4{bflo(b1a[2]), bfhi(b1a[2]), bflo(b1a[3]), bfhi(b1a[3])};
    if (lane < 8) {
      *(LAS f32x4*)(smem + BIAS_OFF + 2048 + lane * 32) = f32x4{bflo(b1b[0]), bfhi(b1b[0]), bflo(b1b[1]), bfhi(b1b[1])};
      *(LAS f32x4*)(smem + BIAS_OFF + 2048 + lane * 32 + 16) = f32x4{bflo(b1b[2]), bfhi(b1b[2]), bflo(b1b[3]), bfhi(b1b[3])};
    }
    const u64 tag = (((u64)(unsigned)__builtin_amdgcn_readfirstlane(ep[1]) << 32) | (unsigned)__builtin_amdgcn_readfirstlane(ep[0])) + 16;
    service(p, smem, rws, lane, r, j, tag, rp, rpo, stamp_);
  }
}

extern "C" void kernel_launch(void* const* d_in, const int* in_sizes, int n_in, void* d_out, int out_size, void* d_ws, size_t ws_size, hipStream_t stream) {
  Params p;
  p.x = (const u16*)d_in[0]; p.wqkv = (const u16*)d_in[1]; p.wo = (const u16*)d_in[2]; p.w1 = (const u16*)d_in[4]; p.b1 = (const u16*)d_in[5];
  p.w2 = (const u16*)d_in[6]; p.b2 = (const u16*)d_in[7]; p.out = (u16*)d_out; p.ws = (char*)d_ws;
  (void)hipFuncSetAttribute((const void*)fused_main, hipFuncAttributeMaxDynamicSharedMemorySize, LDS_BYTES);
  fused_main<<<256, 512, LDS_BYTES, stream>>>(p);
}
```
